```python
import torch

def _softmax(x, axis=-1):
    return torch.softmax(x, dim=axis)
def _gelu(x):
    return torch.nn.functional.gelu(x, approximate="tanh")
def _silu(x):
    return torch.nn.functional.silu(x)
def _sigmoid(x):
    return torch.sigmoid(x)
def _layernorm(x, axis=-1, eps=1e-5):
    m = x.mean(dim=axis, keepdim=True)
    v = x.var(dim=axis, keepdim=True, unbiased=False)
    return (x - m) / torch.sqrt(v + eps)
def _scatter_add(tgt, idx, src, axis=0):
    return tgt.clone().index_add_(axis, idx, src)
def _segment_sum(src, seg, n):
    out = torch.zeros((n,) + src.shape[1:], dtype=src.dtype)
    return out.index_add_(0, seg, src)
def _pad(x, pw):
    flat = tuple(v for pair in reversed(pw) for v in pair)
    return torch.nn.functional.pad(x, flat)

kernel_name = 'parallel_sum__sliding_local_dilated__dense_activation_squared_relu__prompt'

def setup_inputs():

    _w = (lambda a: (lambda t: t.to(torch.bfloat16) if t.is_floating_point() else t)(torch.as_tensor(a)))
    def _A_setup():
        B, S, D = 1, 2048, 448
        return {
            "x": _w((lambda *s: torch.randn(*s, dtype=torch.bfloat16) * 0.05)(B, S, D)),
            "wqkv": _w((lambda *s: torch.randn(*s, dtype=torch.bfloat16) * 0.1)(D, 3 * D)),
            "wo": _w((lambda *s: torch.randn(*s, dtype=torch.bfloat16) * 0.05)(D, D)),
        }
    _A_in = _A_setup()

    def _B_setup():
        B, S, D, F = 1, 2048, 448, 1792
        return {
            "x": _w((lambda *s: torch.randn(*s, dtype=torch.bfloat16) * 0.05)(B, S, D)),
            "w1": _w((lambda *s: torch.randn(*s, dtype=torch.bfloat16) * 0.05)(D, F)),
            "b1": _w((lambda *s: torch.randn(*s, dtype=torch.bfloat16) * 0.2)(F)),
            "w2": _w((lambda *s: torch.randn(*s, dtype=torch.bfloat16) * 0.05)(F, D)),
            "b2": _w((lambda *s: torch.randn(*s, dtype=torch.bfloat16) * 0.8)(D)),
        }
    _B_in = _B_setup()

    return {
        "A_x": _A_in["x"],
        "A_wqkv": _A_in["wqkv"],
        "A_wo": _A_in["wo"],
        "B_x": _B_in["x"],
        "B_w1": _B_in["w1"],
        "B_b1": _B_in["b1"],
        "B_w2": _B_in["w2"],
        "B_b2": _B_in["b2"],
    }

def reference(A_x, A_wqkv, A_wo, B_x, B_w1, B_b1, B_w2, B_b2):
    def _A_ref(x, wqkv, wo):
        B, S, D = x.shape
        H = 16; Dh = D // H
        W, dil = 8, 2
        qkv = (lambda x, *d: x.permute(*d))((x @ wqkv).reshape(B, S, 3, H, Dh), 2, 0, 3, 1, 4)
        q, k, v = qkv[0], qkv[1], qkv[2]
        scale = Dh ** -0.5
        pos = torch.arange(S)
        off = torch.arange(W) * dil
        idx = pos[:, None] - off[None, :]
        mask = idx >= 0
        idx_c = torch.where(mask, idx, 0).reshape(-1)
        kg = k[:, :, idx_c, :].reshape(B, H, S, W, Dh)
        vg = v[:, :, idx_c, :].reshape(B, H, S, W, Dh)
        logits = torch.einsum("bhsd,bhswd->bhsw", q, kg) * scale
        logits = torch.where(mask[None, None, :, :], logits, -1e9)
        attn = _softmax(logits, axis=-1)
        o = torch.einsum("bhsw,bhswd->bhsd", attn, vg)
        return (lambda x, *d: x.permute(*d))(o, 0, 2, 1, 3).reshape(B, S, D) @ wo

    def _B_ref(x, w1, b1, w2, b2):
        h = torch.einsum("bsd,df->bsf", x, w1) + b1
        zero = torch.zeros_like(h)
        r = torch.where(h > zero, h, zero)
        h = r * r
        out = torch.einsum("bsf,fd->bsd", h, w2) + b2
        return out

    x = A_x
    xn = _layernorm(x, axis=-1)
    return x + _A_ref(xn, A_wqkv, A_wo) + _B_ref(xn, B_w1, B_b1, B_w2, B_b2)
```

```cpp
#include <hip/hip_runtime.h>
#include <hip/hip_bf16.h>
typedef unsigned short u16;
__device__ __forceinline__ float b2f(u16 x){ return __uint_as_float(((unsigned)x)<<16); }
__device__ __forceinline__ u16 f2b(float f){ __hip_bfloat16 b = __float2bfloat16(f); return *(u16*)&b; }
constexpr int S=2048, D=448, F=1792, H=16, DH=28;

__global__ void k_ln(const u16* x, u16* xn){
  int r=blockIdx.x; int l=threadIdx.x; float v[7]; float s=0;
  for(int i=0;i<7;i++){ v[i]=b2f(x[r*D+l+64*i]); s+=v[i]; }
  for(int m=32;m>=1;m>>=1) s+=__shfl_xor(s,m);
  float mean=s/D; float q=0;
  for(int i=0;i<7;i++){ float d=v[i]-mean; q+=d*d; }
  for(int m=32;m>=1;m>>=1) q+=__shfl_xor(q,m);
  float rs=rsqrtf(q/D+1e-5f);
  for(int i=0;i<7;i++) xn[r*D+l+64*i]=f2b((v[i]-mean)*rs);
}
__global__ void k_g1(const u16* xn,const u16* wqkv,const u16* w1,const u16* b1,u16* qkv,u16* h){
  int n=blockIdx.x*64+threadIdx.x; int r=blockIdx.y;
  float acc=0;
  if(n<3*D){ for(int k=0;k<D;k++) acc+=b2f(xn[r*D+k])*b2f(wqkv[k*3*D+n]); qkv[r*3*D+n]=f2b(acc); }
  else { int f=n-3*D; for(int k=0;k<D;k++) acc+=b2f(xn[r*D+k])*b2f(w1[k*F+f]); acc+=b2f(b1[f]); acc=fmaxf(acc,0.f); h[r*F+f]=f2b(acc*acc); }
}
__global__ void k_attn(const u16* qkv,u16* o){
  int idx=blockIdx.x*64+threadIdx.x; int r=idx/H, hh=idx%H;
  float lg[8]; float mx=-1e30f;
  for(int w=0;w<8;w++){ int j=r-2*w; float a=0; if(j>=0){ for(int d=0;d<DH;d++) a+=b2f(qkv[r*3*D+hh*DH+d])*b2f(qkv[j*3*D+D+hh*DH+d]); a*=0.18898223650461362f; } else a=-1e9f; lg[w]=a; mx=fmaxf(mx,a);}
  float s=0; for(int w=0;w<8;w++){ lg[w]=__expf(lg[w]-mx); s+=lg[w]; }
  for(int d=0;d<DH;d++){ float a=0; for(int w=0;w<8;w++){ int j=r-2*w; if(j<0) j=0; a+=lg[w]*b2f(qkv[j*3*D+2*D+hh*DH+d]); } o[r*D+hh*DH+d]=f2b(a/s); }
}
__global__ void k_g2(const u16* x,const u16* o,const u16* h,const u16* wo,const u16* w2,const u16* b2,u16* out){
  int n=blockIdx.x*64+threadIdx.x; int r=blockIdx.y; float acc=0;
  for(int k=0;k<D;k++) acc+=b2f(o[r*D+k])*b2f(wo[k*D+n]);
  for(int k=0;k<F;k++) acc+=b2f(h[r*F+k])*b2f(w2[k*D+n]);
  out[r*D+n]=f2b(acc+b2f(b2[n])+b2f(x[r*D+n]));
}
extern "C" void kernel_launch(void* const* d_in, const int* in_sizes, int n_in, void* d_out, int out_size, void* d_ws, size_t ws_size, hipStream_t stream){
  const u16* x=(const u16*)d_in[0]; const u16* wqkv=(const u16*)d_in[1]; const u16* wo=(const u16*)d_in[2];
  const u16* w1=(const u16*)d_in[4]; const u16* b1=(const u16*)d_in[5]; const u16* w2=(const u16*)d_in[6]; const u16* b2=(const u16*)d_in[7];
  u16* ws=(u16*)d_ws; u16* xn=ws; u16* qkv=xn+S*D; u16* h=qkv+S*3*D; u16* o=h+S*F;
  k_ln<<<S,64,0,stream>>>(x,xn);
  k_g1<<<dim3((3*D+F)/64,S),64,0,stream>>>(xn,wqkv,w1,b1,qkv,h);
  k_attn<<<S*H/64,64,0,stream>>>(qkv,o);
  k_g2<<<dim3(D/64,S),64,0,stream>>>(x,o,h,wo,w2,b2,(u16*)d_out);
}
```

```cpp
#include <hip/hip_runtime.h>
#include <hip/hip_bf16.h>

typedef __bf16 bf16x2 __attribute__((ext_vector_type(2)));
typedef __bf16 bf16x4 __attribute__((ext_vector_type(4)));
typedef __bf16 bf16x8 __attribute__((ext_vector_type(8)));
typedef float f32x2 __attribute__((ext_vector_type(2)));
typedef float f32x4 __attribute__((ext_vector_type(4)));
typedef unsigned u32x2 __attribute__((ext_vector_type(2)));
typedef unsigned u32x4 __attribute__((ext_vector_type(4)));
typedef unsigned long long u64;
#define LAS __attribute__((address_space(3)))
#define DEV __device__ __forceinline__

constexpr int kLds = 163840;
constexpr int kSlot = 45056;
constexpr int kAtt = 98304;
constexpr int kCex = kAtt + 144 * 400;
constexpr int kWsFlags = 4096, kWsStats = 65536, kWsA2 = 131072, kWsDiag = 16 << 20;
constexpr int kSpinCap = 1 << 16;

#define STAMP(i)
#define STAMP_ARG
#define STAMP_PASS
#define STAMP_OUT()

DEV void glds(unsigned voff, const char* sbase, unsigned dst) {
  asm volatile("s_mov_b32 m0, %2\n\ts_nop 0\n\tglobal_load_lds_dwordx4 %0, %1" ::"v"(voff), "s"(sbase), "s"(dst) : "memory");
}
DEV void store_wt(unsigned voff, const char* sbase, u32x4 v) {
  asm volatile("global_store_dwordx4 %0, %1, %2 sc1\n\ts_nop 1" ::"v"(voff), "v"(v), "s"(sbase) : "memory");
}
#define WAIT_VM(n) asm volatile("s_waitcnt vmcnt(" #n ")" ::: "memory")
#define WAIT_LGKM0() asm volatile("s_waitcnt lgkmcnt(0)" ::: "memory")
#define BARRIER() do { asm volatile("" ::: "memory"); __builtin_amdgcn_s_barrier(); asm volatile("" ::: "memory"); } while (0)

DEV bf16x8 lds_b128(unsigned a) { return *(LAS bf16x8*)(size_t)a; }
DEV f32x4 lds_f4(unsigned a) { return *(LAS f32x4*)(size_t)a; }
DEV bf16x8 lds_tr(unsigned a0, unsigned a1) {
  bf16x4 a = __builtin_amdgcn_ds_read_tr16_b64_v4bf16((LAS bf16x4*)(size_t)a0);
  bf16x4 b = __builtin_amdgcn_ds_read_tr16_b64_v4bf16((LAS bf16x4*)(size_t)a1);
  return __builtin_shufflevector(a, b, 0, 1, 2, 3, 4, 5, 6, 7);
}
DEV f32x4 mfma(bf16x8 a, bf16x8 b, f32x4 c) { return __builtin_amdgcn_mfma_f32_16x16x32_bf16(a, b, c, 0, 0, 0); }
DEV unsigned pack2(float lo, float hi) { f32x2 v = {lo, hi}; return __builtin_bit_cast(unsigned, __builtin_convertvector(v, bf16x2)); }
DEV float blo(unsigned u) { return __uint_as_float(u << 16); }
DEV float bhi(unsigned u) { return __uint_as_float(u & 0xffff0000u); }
template <int C> DEV float dpp(float x) { return __builtin_bit_cast(float, __builtin_amdgcn_update_dpp(0, __builtin_bit_cast(int, x), C, 0xF, 0xF, true)); }
DEV float x16_add(float x) { auto r = __builtin_amdgcn_permlane16_swap(__float_as_uint(x), __float_as_uint(x), false, false); return __uint_as_float(r[0]) + __uint_as_float(r[1]); }
DEV float x32_add(float x) { auto r = __builtin_amdgcn_permlane32_swap(__float_as_uint(x), __float_as_uint(x), false, false); return __uint_as_float(r[0]) + __uint_as_float(r[1]); }
DEV float x16_max(float x) { auto r = __builtin_amdgcn_permlane16_swap(__float_as_uint(x), __float_as_uint(x), false, false); return fmaxf(__uint_as_float(r[0]), __uint_as_float(r[1])); }
DEV float x32_max(float x) { auto r = __builtin_amdgcn_permlane32_swap(__float_as_uint(x), __float_as_uint(x), false, false); return fmaxf(__uint_as_float(r[0]), __uint_as_float(r[1])); }
DEV float wave_sum(float x) { x += dpp<0xB1>(x); x += dpp<0x4E>(x); x += dpp<0x141>(x); x += dpp<0x140>(x); return x32_add(x16_add(x)); }
DEV int swap23(int r) { return (r & ~12) | ((r & 4) << 1) | ((r & 8) >> 1); }

template <int NM, int NR> DEV void interleave() {
#pragma unroll
  for (int q = 0; q < NM; ++q) {
    __builtin_amdgcn_sched_group_barrier(0x8, 1, 0);
    if (q * 2 + 1 < NR) __builtin_amdgcn_sched_group_barrier(0x100, 2, 0);
    else if (q * 2 < NR) __builtin_amdgcn_sched_group_barrier(0x100, 1, 0);
  }
}

template <bool FFN>
DEV void phase1(const char* x, const char* wmat, const char* b1, char* ws, u64& tag64, int lane, int w, int rt, int cg, int k0,
                unsigned v2, const char* w2s0, const char* w2s1, const char* w2s2 STAMP_ARG) {
  constexpr int NT = FFN ? 7 : 6;
  constexpr int WOFF = FFN ? 16384 : 18432, WROW = FFN ? 448 : 384, CPR = FFN ? 28 : 24, GROW = FFN ? 3584 : 2688;
  const int mw = w & 3, nw = w >> 2, m = lane & 15, g = lane >> 4, q = m >> 2, p = lane & 3;
  const int r0 = rt * 128;
  u32x2 ep;
  asm volatile("global_load_dwordx2 %0, %1, %2 sc1" : "=v"(ep) : "v"(0), "s"(ws) : "memory");
  unsigned xo[2], wo[3], oo;
#pragma unroll
  for (int u = 0; u < 2; ++u) {
    const int sg = (2 * w + u) * 8 + (lane >> 3), t = sg >> 4;
    xo[u] = (r0 + 32 * (t >> 1) + 2 * (sg & 15) + (t & 1)) * 896 + (((lane & 7) ^ ((sg >> 1) & 7)) << 4);
  }
  auto woff = [&](int i) -> unsigned {
    const int L = i * 64 + lane, rho = L / CPR, c1 = L - rho * CPR, k = swap23(rho);
    if (FFN) return k * GROW + cg * 448 + ((c1 ^ ((rho >> 1) & 2)) << 4);
    const int c = c1 ^ (rho & 6), cc = (c & 7) < 6 ? (c & 7) : 6;
    return k * GROW + (c >> 3) * 896 + cg * 112 + (cc << 4);
  };
#pragma unroll
  for (int u = 0; u < 3; ++u) wo[u] = woff(3 * w + u);
  if (FFN) oo = woff(24 + (w & 3));
  else { const int sg = 128 + (w & 1) * 8 + (lane >> 3); int r = r0 - 16 + (sg & 15); r = r < 0 ? 0 : r; oo = r * 896 + (((lane & 7) ^ ((sg >> 1) & 7)) << 4); }
  const bool opt = FFN ? w < 4 : w < 2;
  int ks = k0;
  auto stage = [&](unsigned slot) {
    const char* xb = x + ks * 128;
    const char* wb = wmat + ks * (64 * GROW);
    glds(xo[0], xb, slot + w * 2048);
    glds(wo[0], wb, slot + WOFF + w * 3072);
    glds(xo[1], xb, slot + w * 2048 + 1024);
    glds(wo[1], wb, slot + WOFF + w * 3072 + 1024);
    glds(wo[2], wb, slot + WOFF + w * 3072 + 2048);
    if (opt) glds(oo, FFN ? wb : xb, FFN ? slot + WOFF + (24 + w) * 1024 : slot + (16 + w) * 1024);
    ks = ks == 6 ? 0 : ks + 1;
  };
  u32x2 bias[7];
  u32x4 srow;
  const int strow = blockIdx.x * 8 + w;
  asm volatile("global_load_dwordx4 %0, %1, %2" : "=v"(srow) : "v"(strow * 896 + (lane < 56 ? lane : 55) * 16), "s"(x) : "memory");
  if (FFN) {
#pragma unroll
    for (int n = 0; n < 7; ++n)
      asm volatile("global_load_dwordx2 %0, %1, %2 offset:%3" : "=v"(bias[n]) : "v"((cg * 224 + nw * 112 + 4 * g) * 2), "s"(b1), "i"(n * 32) : "memory");
  }
  stage(0);
  stage(kSlot);
  if (FFN) asm volatile("s_waitcnt vmcnt(17)" : "+v"(srow), "+v"(ep)::"memory"); else asm volatile("s_waitcnt vmcnt(10)" : "+v"(srow), "+v"(ep)::"memory");
  tag64 = (((u64)ep[1] << 32) | ep[0]) + 1;
  const unsigned tag = (unsigned)tag64;
  {
    float v[8];
#pragma unroll
    for (int i = 0; i < 4; ++i) { v[2 * i] = blo(srow[i]); v[2 * i + 1] = bhi(srow[i]); }
    float s = ((v[0] + v[1]) + (v[2] + v[3])) + ((v[4] + v[5]) + (v[6] + v[7]));
    const float mean = wave_sum(lane < 56 ? s : 0.f) * (1.f / 448.f);
    float d = 0.f;
#pragma unroll
    for (int i = 0; i < 8; ++i) { const float e = v[i] - mean; d = fmaf(e, e, d); }
    const float var = wave_sum(lane < 56 ? d : 0.f) * (1.f / 448.f);
    const float rstd = __builtin_amdgcn_rsqf(var + 1e-5f);
    if (lane == 0) {
      const u32x4 gr = {__float_as_uint(mean), tag, __float_as_uint(rstd), tag};
      store_wt(kWsStats + strow * 16, ws, gr);
    }
  }
  unsigned ax[2], aw[4];
#pragma unroll
  for (int kk = 0; kk < 2; ++kk) ax[kk] = (32 * mw + m) * 128 + (((4 * kk + g) ^ (m >> 1)) << 4);
  {
    const int rho = 4 * (g & 1) + q + 16 * (g >> 1);
    const unsigned base = WOFF + rho * WROW + (p >> 1) * 16 + (p & 1) * 8;
#pragma unroll
    for (int i = 0; i < 4; ++i) {
      if (FFN) aw[i] = base + (((7 * nw + i) ^ (g & 1)) - i) * 32;
      else aw[i] = base + ((6 * nw + i) ^ (2 * (g & 1) + (q >> 1))) * 32;
    }
  }
  const unsigned exoff = (8 - 2 * mw) * 2048;
  f32x4 acc[2][NT] = {}, ace[NT] = {};
  bf16x8 X[2][2], W[2][NT], XE[2];
  auto rd = [&](int kk, unsigned slot) {
#pragma unroll
    for (int mt = 0; mt < 2; ++mt) X[kk][mt] = lds_b128(slot + ax[kk] + mt * 2048);
#pragma unroll
    for (int n = 0; n < NT; ++n) {
      const unsigned a = slot + (FFN ? aw[n & 1] + n * 32 : aw[n & 3] + (n >> 2) * 128) + kk * 32 * WROW;
      W[kk][n] = lds_tr(a, a + 8 * WROW);
    }
    if (!FFN) XE[kk] = lds_b128(slot + ax[kk] + exoff);
  };
  auto mm = [&](int kk) {
#pragma unroll
    for (int n = 0; n < NT; ++n)
#pragma unroll
      for (int mt = 0; mt < 2; ++mt) acc[mt][n] = mfma(W[kk][n], X[kk][mt], acc[mt][n]);
  };
  auto extra = [&](int kk) {
    const u32x4 one = {0x3f803f80u, 0x3f803f80u, 0x3f803f80u, 0x3f803f80u};
    u32x4 e = one;
    if (!FFN) { const u32x4 h = __builtin_bit_cast(u32x4, XE[kk]); e = m == 0 ? one : h; }
#pragma unroll
    for (int n = 0; n < NT; ++n) ace[n] = mfma(W[kk][n], __builtin_bit_cast(bf16x8, e), ace[n]);
  };
  const bool ex0 = mw == 2 * nw, ex1 = FFN ? mw == 2 * nw + 1 : ex0;
  u32x4 st[3];
  auto ldstats = [&]() {
#pragma unroll
    for (int mt = 0; mt < 2; ++mt)
      asm volatile("global_load_dwordx4 %0, %1, %2 offset:%3 sc1" : "=v"(st[mt]) : "v"(kWsStats + (r0 + 32 * mw + 2 * m) * 16), "s"(ws), "i"(mt * 16) : "memory");
    if (!FFN) { const int r = r0 - 16 + m; asm volatile("global_load_dwordx4 %0, %1, %2 sc1" : "=v"(st[2]) : "v"(kWsStats + (r < 0 ? 0 : r) * 16), "s"(ws) : "memory"); }
  };

  WAIT_VM(6);
  BARRIER();
  STAMP(1);
  rd(0, 0);
  stage(2 * kSlot);
  unsigned cur = 0;
#pragma nounroll
  for (int s = 0; s < 6; ++s) {
    rd(1, cur);
    mm(0);
    interleave<2 * NT, 2 + 2 * NT + (FFN ? 0 : 1)>();
    if (ex0) extra(0);
    WAIT_LGKM0();
    if (s < 5) WAIT_VM(5); else WAIT_VM(0);
    BARRIER();
    if (s < 4) stage(cur);
    cur = cur == 2 * kSlot ? 0 : cur + kSlot;
    rd(0, cur);
    mm(1);
    interleave<2 * NT, 2 + 2 * NT + (FFN ? 0 : 1)>();
    if (ex1) extra(1);
  }
  ldstats();
  rd(1, 0);
  mm(0);
  interleave<2 * NT, 2 + 2 * NT + (FFN ? 0 : 1)>();
  if (ex0) extra(0);
  WAIT_LGKM0();
  BARRIER();
#pragma unroll
  for (int i = 0; i < 4; ++i) glds(v2, w2s0 + (16 * (i >> 1) + 4 * (i & 1)) * 896, w * 12288 + i * 1024);
#pragma unroll
  for (int i = 0; i < 4; ++i) glds(v2, w2s1 + (16 * (i >> 1) + 4 * (i & 1)) * 896, w * 12288 + 4096 + i * 1024);
#pragma unroll
  for (int i = 0; i < 4; ++i) glds(v2, w2s2 + (16 * (i >> 1) + 4 * (i & 1)) * 896, w * 12288 + 8192 + i * 1024);
  mm(1);
  if (ex1) extra(1);
  STAMP(2);
  if (m == 0 && (ex0 || ex1)) {
#pragma unroll
    for (int n = 0; n < NT; ++n) *(LAS f32x4*)(size_t)(kCex + ((FFN ? (mw & 1) * 224 : 0) + (nw * NT + n) * 16 + 4 * g) * 4) = ace[n];
  }
  if (FFN) asm volatile("s_waitcnt vmcnt(12)" : "+v"(st[0]), "+v"(st[1])::"memory");
  else asm volatile("s_waitcnt vmcnt(12)" : "+v"(st[0]), "+v"(st[1]), "+v"(st[2])::"memory");
#pragma nounroll
  for (int spin = 0; spin < kSpinCap; ++spin) {
    bool ok = st[0][1] == tag && st[0][3] == tag && st[1][1] == tag && st[1][3] == tag;
    if (!FFN) ok = ok && st[2][1] == tag && st[2][3] == tag;
    if (__all(ok)) break;
    __builtin_amdgcn_s_sleep(2);
    ldstats();
    if (FFN) asm volatile("s_waitcnt vmcnt(0)" : "+v"(st[0]), "+v"(st[1])::"memory");
    else asm volatile("s_waitcnt vmcnt(0)" : "+v"(st[0]), "+v"(st[1]), "+v"(st[2])::"memory");
  }
  WAIT_LGKM0();
  BARRIER();
  const char* a2 = ws + kWsA2;
  if (FFN) {
#pragma unroll
    for (int n = 0; n < 7; ++n) asm volatile("" : "+v"(bias[n]));
    const float rs0 = __uint_as_float(st[0][2]), rs1 = __uint_as_float(st[1][2]);
    const float mu0 = -rs0 * __uint_as_float(st[0][0]), mu1 = -rs1 * __uint_as_float(st[1][0]);
    const unsigned sto = ((2 * rt + (mw >> 1)) * 72 + 7 * cg) * 4096 + (2 * (mw & 1) + (g & 1)) * 1024 + ((g >> 1) * 16 + m) * 16;
    f32x4 cs[7];
#pragma unroll
    for (int n = 0; n < 7; ++n) { const unsigned ca = kCex + ((nw * 7 + n) * 16 + 4 * g) * 4; cs[n] = lds_f4(ca) + lds_f4(ca + 896); }
#pragma unroll
    for (int n = 0; n < 7; ++n) {
      const f32x4 c = cs[n];
      const float b[4] = {blo(bias[n][0]), bhi(bias[n][0]), blo(bias[n][1]), bhi(bias[n][1])};
      float y0[4], y1[4];
#pragma unroll
      for (int r = 0; r < 4; ++r) {
        const float u0 = fmaf(rs0, acc[0][n][r], fmaf(mu0, c[r], b[r])), u1 = fmaf(rs1, acc[1][n][r], fmaf(mu1, c[r], b[r]));
        const float t0 = fmaxf(u0, 0.f), t1 = fmaxf(u1, 0.f);
        y0[r] = t0 * t0; y1[r] = t1 * t1;
      }
      const auto s0 = __builtin_amdgcn_permlane16_swap(pack2(y0[0], y0[1]), pack2(y1[0], y1[1]), false, false);
      const auto s1 = __builtin_amdgcn_permlane16_swap(pack2(y0[2], y0[3]), pack2(y1[2], y1[3]), false, false);
      const u32x4 v = {s0[0], s1[0], s0[1], s1[1]};
      const int T = 7 * nw + n;
      store_wt(sto, a2 + (T >> 1) * 4096 + (T & 1) * 512, v);
    }
  } else {
    unsigned co[4];
#pragma unroll
    for (int i = 0; i < 4; ++i) {
      const int T = 6 * nw + i, n = 16 * (T & 3) + 4 * g;
      co[i] = kAtt + (T >> 2) * 128 + (n < 28 ? n : n < 56 ? n + 4 : n < 60 ? n - 28 : n) * 2;
    }
    f32x4 cs[NT];
#pragma unroll
    for (int n = 0; n < NT; ++n) cs[n] = lds_f4(kCex + ((nw * 6 + n) * 16 + 4 * g) * 4);
    auto put = [&](const f32x4 (&a)[NT], const u32x4& s, unsigned row) {
      const float mean = __uint_as_float(s[0]), rs = __uint_as_float(s[2]);
#pragma unroll
      for (int n = 0; n < NT; ++n) {
        const f32x4 c = cs[n];
        u32x2 v = {pack2(rs * fmaf(-mean, c[0], a[n][0]), rs * fmaf(-mean, c[1], a[n][1])), pack2(rs * fmaf(-mean, c[2], a[n][2]), rs * fmaf(-mean, c[3], a[n][3]))};
        if (((6 * nw + n) & 3) == 3 && g >= 2) v = u32x2{0u, 0u};
        *(LAS u32x2*)(size_t)(co[n & 3] + (n >> 2) * 128 + row * 400) = v;
      }
    };
    put(acc[0], st[0], 8 + 16 * mw + m);
    put(acc[1], st[1], 72 + 8 + 16 * mw + m);
    if (ex0) put(ace, st[2], (m & 1) * 72 + (m >> 1));
    WAIT_LGKM0();
    BARRIER();
    const int blk = w >> 1, par = w & 1;
    const unsigned rb = kAtt + (par * 72 + 16 * blk) * 400;
    const unsigned aq = rb + (m + 8) * 400 + 16 * g, ak0 = rb + m * 400 + 128 + 16 * g, ak1 = rb + (16 + (m & 7)) * 400 + 128 + 16 * g;
    const unsigned av0 = rb + (4 * g + q) * 400 + 256 + 16 * p, av1 = rb + (16 + 4 * (g & 1) + q) * 400 + 256 + 16 * p;
    const int jmin = (16 - (r0 + 32 * blk + par) + 1) >> 1;
    const float kLog = 0.18898223650461362f * 1.4426950408889634f;
#pragma unroll
    for (int hd = 0; hd < 2; ++hd) {
      const bf16x8 qf = lds_b128(aq + hd * 64);
      const f32x4 z = {0.f, 0.f, 0.f, 0.f};
      const f32x4 s0 = mfma(lds_b128(ak0 + hd * 64), qf, z), s1 = mfma(lds_b128(ak1 + hd * 64), qf, z);
      float e[8], mx = -3.0e38f;
#pragma unroll
      for (int i = 0; i < 8; ++i) {
        const int j = 16 * (i >> 2) + 4 * g + (i & 3);
        const float v = j < jmin ? -1.0e9f * 1.4426950408889634f : (i < 4 ? s0[i & 3] : s1[i & 3]) * kLog;
        e[i] = (unsigned)(m + 8 - j) < 8u ? v : -__builtin_inff();
        mx = fmaxf(mx, e[i]);
      }
      mx = x32_max(x16_max(mx));
      float sum = 0.f;
#pragma unroll
      for (int i = 0; i < 8; ++i) { e[i] = __builtin_amdgcn_exp2f(e[i] - mx); sum += e[i]; }
      const float inv = __builtin_amdgcn_rcpf(x32_add(x16_add(sum)));
      const u32x4 pf = {pack2(e[0] * inv, e[1] * inv), pack2(e[2] * inv, e[3] * inv), pack2(e[4] * inv, e[5] * inv), pack2(e[6] * inv, e[7] * inv)};
      const f32x4 o0 = mfma(lds_tr(av0 + hd * 64, av1 + hd * 64), __builtin_bit_cast(bf16x8, pf), z);
      const f32x4 o1 = mfma(lds_tr(av0 + hd * 64 + 8, av1 + hd * 64 + 8), __builtin_bit_cast(bf16x8, pf), z);
      const u32x4 v = {pack2(o0[0], o0[1]), pack2(o0[2], o0[3]), pack2(o1[0], o1[1]), pack2(o1[2], o1[3])};
      store_wt(lane * 16, a2 + (((2 * rt + (blk >> 1)) * 72 + 56 + 2 * cg + hd) * 4 + 2 * (blk & 1) + par) * 1024, v);
    }
  }
  STAMP(3);
  asm volatile("s_waitcnt vmcnt(0) lgkmcnt(0)" ::: "memory");
  BARRIER();
  if (w == 0 && lane == 0)
    asm volatile("global_store_dwordx2 %0, %1, %2 sc1" ::"v"(kWsFlags + (rt * 16 + cg + (FFN ? 0 : 8)) * 128), "v"(tag64), "s"(ws) : "memory");
  STAMP(4);
}

__global__ __launch_bounds__(512, 2) void fused_kernel(const char* x, const char* wqkv, const char* wo, const char* w1, const char* b1, const char* w2,
                                                       const char* b2, char* out, char* ws) {
  __shared__ __attribute__((aligned(16))) char smem[kLds];
  asm volatile("" ::"v"((LAS char*)smem));
  const int lane = threadIdx.x & 63, w = __builtin_amdgcn_readfirstlane(threadIdx.x >> 6);
  const int bid = blockIdx.x, xcd = bid & 7, j = bid >> 3;
  STAMP(0);
  const int rt = (xcd & 1) * 8 + (j & 7), cgi = j >> 3, cg = 2 * (xcd >> 1) + (cgi & 1);
  const int k0 = (rt + 2 * cgi + (xcd >> 1)) % 7;
  const int jj = j < 28 ? j : 27, rg = 4 * xcd + jj / 7, ct = jj % 7, rot = 2 * (rg & 3);
  const int r7 = lane >> 3;
  const unsigned v2 = (8 * (r7 >> 2) + (r7 & 3)) * 896 + (((lane & 7) ^ (r7 & 6)) << 4);
  const char* w2w = w2 + w * (224 * 896) + ct * 128;
  auto hstep = [&](int i) { const int s = i + rot; return s >= 7 ? s - 7 : s; };

  u64 tag64;
  if (cgi < 2) phase1<true>(x, w1, b1, ws, tag64, lane, w, rt, cg, k0, v2, w2w + hstep(0) * (32 * 896), w2w + hstep(1) * (32 * 896), w2w + hstep(2) * (32 * 896) STAMP_PASS);
  else phase1<false>(x, wqkv, b1, ws, tag64, lane, w, rt, cg, k0, v2, w2w + hstep(0) * (32 * 896), w2w + hstep(1) * (32 * 896), w2w + hstep(2) * (32 * 896) STAMP_PASS);

  if (j >= 28) {
    if (bid == 255 && w == 0) {
#pragma nounroll
      for (int spin = 0; spin < kSpinCap; ++spin) {
        u32x2 a, b, c, d;
        asm volatile("global_load_dwordx2 %0, %4, %5 sc1\n\tglobal_load_dwordx2 %1, %4, %6 sc1\n\tglobal_load_dwordx2 %2, %4, %7 sc1\n\tglobal_load_dwordx2 %3, %4, %8 sc1\n\ts_waitcnt vmcnt(0)"
                     : "=&v"(a), "=&v"(b), "=&v"(c), "=&v"(d)
                     : "v"(lane * 128), "s"(ws + kWsFlags), "s"(ws + kWsFlags + 8192), "s"(ws + kWsFlags + 16384), "s"(ws + kWsFlags + 24576) : "memory");
        const unsigned t0 = (unsigned)tag64, t1 = (unsigned)(tag64 >> 32);
        if (__all(a[0] == t0 && b[0] == t0 && c[0] == t0 && d[0] == t0 && a[1] == t1 && b[1] == t1 && c[1] == t1 && d[1] == t1)) break;
        __builtin_amdgcn_s_sleep(2);
      }
      if (lane == 0) asm volatile("global_store_dwordx2 %0, %1, %2 sc1" ::"v"(0), "v"(tag64), "s"(ws) : "memory");
    }
    STAMP_OUT();
    return;
  }

  const int m = lane & 15, g = lane >> 4, q = m >> 2, p = lane & 3;
  unsigned aw[4];
#pragma unroll
  for (int u = 0; u < 4; ++u) aw[u] = (4 * (g & 1) + q + 16 * (g >> 1)) * 128 + ((u ^ (2 * (g & 1) + (q >> 1))) * 32) + (p >> 1) * 16 + (p & 1) * 8;
  const unsigned sA = w * 12288, sB = kAtt + w * 8192;
  bf16x8 Wf[4][4];
  auto rdw = [&](int b, unsigned slot) {
#pragma unroll
    for (int u = 0; u < 4; ++u) Wf[b][u] = lds_tr(slot + aw[u], slot + aw[u] + 1024);
  };
  auto ldw = [&](const char* src, unsigned slot, unsigned vlast) {
#pragma unroll
    for (int i = 0; i < 4; ++i) glds(i == 3 ? vlast : v2, src + (16 * (i >> 1) + 4 * (i & 1)) * 896, slot + i * 1024);
  };
  const int os = threadIdx.x >> 3, orow = rg * 64 + 32 * (os >> 5) + 2 * (os & 15) + ((os >> 4) & 1);
  const unsigned oofs = orow * 896 + ct * 128 + (threadIdx.x & 7) * 16;
  u32x4 xr, br;
  asm volatile("global_load_dwordx4 %0, %1, %2" : "=v"(xr) : "v"(oofs), "s"(x) : "memory");
  asm volatile("global_load_dwordx4 %0, %1, %2" : "=v"(br) : "v"(ct * 128 + (threadIdx.x & 7) * 16), "s"(b2) : "memory");
  rdw(0, sA); rdw(1, sA + 4096); rdw(2, sA + 8192);
  const char* wow = wo + w * (56 * 896) + ct * 128;
  const unsigned v2o = lane < 32 ? v2 : v2 - 4 * 896;
  ldw(w2w + hstep(3) * (32 * 896), sB, v2);
  ldw(w2w + hstep(4) * (32 * 896), sB + 4096, v2);
  WAIT_LGKM0();
  ldw(w2w + hstep(5) * (32 * 896), sA, v2);
  ldw(w2w + hstep(6) * (32 * 896), sA + 4096, v2);
  ldw(wow, sA + 8192, v2o);
#pragma nounroll
  for (int spin = 0; spin < kSpinCap; ++spin) {
    u32x2 f;
    asm volatile("global_load_dwordx2 %0, %1, %2 sc1\n\ts_waitcnt vmcnt(0)" : "=&v"(f) : "v"(kWsFlags + ((rg >> 1) * 16 + w + (lane & 1) * 8) * 128), "s"(ws) : "memory");
    if (__all(f[0] == (unsigned)tag64 && f[1] == (unsigned)(tag64 >> 32))) break;
    __builtin_amdgcn_s_sleep(1);
  }
  STAMP(5);
  const char* a2 = ws + kWsA2 + rg * (72 * 4096);
  u32x4 H[3][4];
  auto ldh = [&](int b, int i) {
    const char* src = a2 + (i < 7 ? 7 * w + hstep(i) : 56 + 2 * w + (i - 7)) * 4096;
#pragma unroll
    for (int mt = 0; mt < 4; ++mt) asm volatile("global_load_dwordx4 %0, %1, %2 offset:%3 sc1" : "=v"(H[b][mt]) : "v"(lane * 16), "s"(src), "i"(mt * 1024) : "memory");
  };
  ldh(0, 0); ldh(1, 1); ldh(2, 2);
  rdw(3, sB);
  WAIT_LGKM0();
  ldw(wow + 28 * 896, sB, v2o);
  f32x4 acc[4][4] = {};
#pragma unroll
  for (int i = 0; i < 9; ++i) {
    const int b = i % 3;
    if (i < 3) asm volatile("s_waitcnt vmcnt(12)" : "+v"(H[b][0]), "+v"(H[b][1]), "+v"(H[b][2]), "+v"(H[b][3])::"memory");
    else if (i < 7) asm volatile("s_waitcnt vmcnt(8)" : "+v"(H[b][0]), "+v"(H[b][1]), "+v"(H[b][2]), "+v"(H[b][3])::"memory");
    else if (i < 8) asm volatile("s_waitcnt vmcnt(4)" : "+v"(H[b][0]), "+v"(H[b][1]), "+v"(H[b][2]), "+v"(H[b][3])::"memory");
    else asm volatile("s_waitcnt vmcnt(0)" : "+v"(H[b][0]), "+v"(H[b][1]), "+v"(H[b][2]), "+v"(H[b][3]), "+v"(xr), "+v"(br)::"memory");
    if (i >= 3 && i < 8) rdw((i + 1) & 3, i == 3 ? sB + 4096 : i == 7 ? sB : sA + (i - 4) * 4096);
#pragma unroll
    for (int mt = 0; mt < 4; ++mt)
#pragma unroll
      for (int u = 0; u < 4; ++u) acc[mt][u] = mfma(Wf[i & 3][u], __builtin_bit_cast(bf16x8, H[b][mt]), acc[mt][u]);
    __builtin_amdgcn_sched_barrier(0);
    if (i + 3 < 9) ldh(b, i + 3);
  }
  STAMP(6);
  WAIT_LGKM0();
  BARRIER();
#pragma unroll
  for (int mt = 0; mt < 4; ++mt)
#pragma unroll
    for (int u = 0; u < 4; ++u) *(LAS f32x4*)(size_t)(w * 16384 + (mt * 16 + m) * 256 + (((4 * u + g) ^ (m & 7)) << 4)) = acc[mt][u];
  WAIT_LGKM0();
  BARRIER();
  f32x4 lo = {blo(xr[0]) + blo(br[0]), bhi(xr[0]) + bhi(br[0]), blo(xr[1]) + blo(br[1]), bhi(xr[1]) + bhi(br[1])};
  f32x4 hi = {blo(xr[2]) + blo(br[2]), bhi(xr[2]) + bhi(br[2]), blo(xr[3]) + blo(br[3]), bhi(xr[3]) + bhi(br[3])};
  const unsigned ra = os * 256 + (((2 * (threadIdx.x & 7)) ^ (os & 7)) << 4);
#pragma unroll
  for (int k = 0; k < 8; ++k) { lo += lds_f4(k * 16384 + ra); hi += lds_f4(k * 16384 + (ra ^ 16)); }
  const u32x4 v = {pack2(lo[0], lo[1]), pack2(lo[2], lo[3]), pack2(hi[0], hi[1]), pack2(hi[2], hi[3])};
  *(u32x4*)(out + oofs) = v;
  STAMP(7);
  STAMP_OUT();
}

extern "C" void kernel_launch(void* const* d_in, const int* in_sizes, int n_in, void* d_out, int out_size, void* d_ws, size_t ws_size, hipStream_t stream) {
  fused_kernel<<<256, 512, 0, stream>>>((const char*)d_in[0], (const char*)d_in[1], (const char*)d_in[2], (const char*)d_in[4], (const char*)d_in[5],
                                        (const char*)d_in[6], (const char*)d_in[7], (char*)d_out, (char*)d_ws);
}
```

```cpp
#include <hip/hip_runtime.h>
#include <hip/hip_bf16.h>

typedef __bf16 bf16x2 __attribute__((ext_vector_type(2)));
typedef __bf16 bf16x4 __attribute__((ext_vector_type(4)));
typedef __bf16 bf16x8 __attribute__((ext_vector_type(8)));
typedef float f32x2 __attribute__((ext_vector_type(2)));
typedef float f32x4 __attribute__((ext_vector_type(4)));
typedef unsigned u32x2 __attribute__((ext_vector_type(2)));
typedef unsigned u32x4 __attribute__((ext_vector_type(4)));
typedef unsigned long long u64;
#define LAS __attribute__((address_space(3)))
#define DEV __device__ __forceinline__

constexpr int kLds = 163840;
constexpr int kSlot = 45056;
constexpr int kAtt = 98304;
constexpr int kCex = kAtt + 144 * 400;
constexpr int kWsFlags = 4096, kWsStats = 65536, kWsA2 = 131072, kWsDiag = 16 << 20;
constexpr int kSpinCap = 1 << 16;

#define PROBE_K 0
#define DIAG_DECL u64 tstamp[10];
#define STAMP(i) do { if ((i) == PROBE_K) asm volatile("s_memrealtime %0\n\ts_waitcnt lgkmcnt(0)" : "=s"(tstamp[0])::"memory"); } while (0)
#define STAMP_ARG , u64 (&tstamp)[10]
#define STAMP_PASS , tstamp
#define STAMP_OUT() do { if (tstamp[0]) { u64 n_; do { __builtin_amdgcn_s_sleep(4); asm volatile("s_memrealtime %0\n\ts_waitcnt lgkmcnt(0)" : "=s"(n_)::"memory"); } while (n_ < tstamp[0] + 6000); } } while (0)

DEV void glds(unsigned voff, const char* sbase, unsigned dst) {
  asm volatile("s_mov_b32 m0, %2\n\ts_nop 0\n\tglobal_load_lds_dwordx4 %0, %1" ::"v"(voff), "s"(sbase), "s"(dst) : "memory");
}
DEV void store_wt(unsigned voff, const char* sbase, u32x4 v) {
  asm volatile("global_store_dwordx4 %0, %1, %2 sc1\n\ts_nop 1" ::"v"(voff), "v"(v), "s"(sbase) : "memory");
}
#define WAIT_VM(n) asm volatile("s_waitcnt vmcnt(" #n ")" ::: "memory")
#define WAIT_LGKM0() asm volatile("s_waitcnt lgkmcnt(0)" ::: "memory")
#define BARRIER() do { asm volatile("" ::: "memory"); __builtin_amdgcn_s_barrier(); asm volatile("" ::: "memory"); } while (0)

DEV bf16x8 lds_b128(unsigned a) { return *(LAS bf16x8*)(size_t)a; }
DEV f32x4 lds_f4(unsigned a) { return *(LAS f32x4*)(size_t)a; }
DEV bf16x8 lds_tr(unsigned a0, unsigned a1) {
  bf16x4 a = __builtin_amdgcn_ds_read_tr16_b64_v4bf16((LAS bf16x4*)(size_t)a0);
  bf16x4 b = __builtin_amdgcn_ds_read_tr16_b64_v4bf16((LAS bf16x4*)(size_t)a1);
  return __builtin_shufflevector(a, b, 0, 1, 2, 3, 4, 5, 6, 7);
}
DEV f32x4 mfma(bf16x8 a, bf16x8 b, f32x4 c) { return __builtin_amdgcn_mfma_f32_16x16x32_bf16(a, b, c, 0, 0, 0); }
DEV unsigned pack2(float lo, float hi) { f32x2 v = {lo, hi}; return __builtin_bit_cast(unsigned, __builtin_convertvector(v, bf16x2)); }
DEV float blo(unsigned u) { return __uint_as_float(u << 16); }
DEV float bhi(unsigned u) { return __uint_as_float(u & 0xffff0000u); }
template <int C> DEV float dpp(float x) { return __builtin_bit_cast(float, __builtin_amdgcn_update_dpp(0, __builtin_bit_cast(int, x), C, 0xF, 0xF, true)); }
DEV float x16_add(float x) { auto r = __builtin_amdgcn_permlane16_swap(__float_as_uint(x), __float_as_uint(x), false, false); return __uint_as_float(r[0]) + __uint_as_float(r[1]); }
DEV float x32_add(float x) { auto r = __builtin_amdgcn_permlane32_swap(__float_as_uint(x), __float_as_uint(x), false, false); return __uint_as_float(r[0]) + __uint_as_float(r[1]); }
DEV float x16_max(float x) { auto r = __builtin_amdgcn_permlane16_swap(__float_as_uint(x), __float_as_uint(x), false, false); return fmaxf(__uint_as_float(r[0]), __uint_as_float(r[1])); }
DEV float x32_max(float x) { auto r = __builtin_amdgcn_permlane32_swap(__float_as_uint(x), __float_as_uint(x), false, false); return fmaxf(__uint_as_float(r[0]), __uint_as_float(r[1])); }
DEV float wave_sum(float x) { x += dpp<0xB1>(x); x += dpp<0x4E>(x); x += dpp<0x141>(x); x += dpp<0x140>(x); return x32_add(x16_add(x)); }
DEV int swap23(int r) { return (r & ~12) | ((r & 4) << 1) | ((r & 8) >> 1); }

template <int NM, int NR> DEV void interleave() {
#pragma unroll
  for (int q = 0; q < NM; ++q) {
    __builtin_amdgcn_sched_group_barrier(0x8, 1, 0);
    if (q * 2 + 1 < NR) __builtin_amdgcn_sched_group_barrier(0x100, 2, 0);
    else if (q * 2 < NR) __builtin_amdgcn_sched_group_barrier(0x100, 1, 0);
  }
}

template <bool FFN>
DEV void phase1(const char* x, const char* wmat, const char* b1, char* ws, u64& tag64, int lane, int w, int rt, int cg, int k0,
                unsigned v2, const char* w2s0, const char* w2s1, const char* w2s2 STAMP_ARG) {
  constexpr int NT = FFN ? 7 : 6;
  constexpr int WOFF = FFN ? 16384 : 18432, WROW = FFN ? 448 : 384, CPR = FFN ? 28 : 24, GROW = FFN ? 3584 : 2688;
  const int mw = w & 3, nw = w >> 2, m = lane & 15, g = lane >> 4, q = m >> 2, p = lane & 3;
  const int r0 = rt * 128;
  u32x2 ep;
  asm volatile("global_load_dwordx2 %0, %1, %2 sc1" : "=v"(ep) : "v"(0), "s"(ws) : "memory");
  unsigned xo[2], wo[3], oo;
#pragma unroll
  for (int u = 0; u < 2; ++u) {
    const int sg = (2 * w + u) * 8 + (lane >> 3), t = sg >> 4;
    xo[u] = (r0 + 32 * (t >> 1) + 2 * (sg & 15) + (t & 1)) * 896 + (((lane & 7) ^ ((sg >> 1) & 7)) << 4);
  }
  auto woff = [&](int i) -> unsigned {
    const int L = i * 64 + lane, rho = L / CPR, c1 = L - rho * CPR, k = swap23(rho);
    if (FFN) return k * GROW + cg * 448 + ((c1 ^ ((rho >> 1) & 2)) << 4);
    const int c = c1 ^ (rho & 6), cc = (c & 7) < 6 ? (c & 7) : 6;
    return k * GROW + (c >> 3) * 896 + cg * 112 + (cc << 4);
  };
#pragma unroll
  for (int u = 0; u < 3; ++u) wo[u] = woff(3 * w + u);
  if (FFN) oo = woff(24 + (w & 3));
  else { const int sg = 128 + (w & 1) * 8 + (lane >> 3); int r = r0 - 16 + (sg & 15); r = r < 0 ? 0 : r; oo = r * 896 + (((lane & 7) ^ ((sg >> 1) & 7)) << 4); }
  const bool opt = FFN ? w < 4 : w < 2;
  int ks = k0;
  auto stage = [&](unsigned slot) {
    const char* xb = x + ks * 128;
    const char* wb = wmat + ks * (64 * GROW);
    glds(xo[0], xb, slot + w * 2048);
    glds(wo[0], wb, slot + WOFF + w * 3072);
    glds(xo[1], xb, slot + w * 2048 + 1024);
    glds(wo[1], wb, slot + WOFF + w * 3072 + 1024);
    glds(wo[2], wb, slot + WOFF + w * 3072 + 2048);
    if (opt) glds(oo, FFN ? wb : xb, FFN ? slot + WOFF + (24 + w) * 1024 : slot + (16 + w) * 1024);
    ks = ks == 6 ? 0 : ks + 1;
  };
  u32x2 bias[7];
  u32x4 srow;
  const int strow = blockIdx.x * 8 + w;
  asm volatile("global_load_dwordx4 %0, %1, %2" : "=v"(srow) : "v"(strow * 896 + (lane < 56 ? lane : 55) * 16), "s"(x) : "memory");
  if (FFN) {
#pragma unroll
    for (int n = 0; n < 7; ++n)
      asm volatile("global_load_dwordx2 %0, %1, %2 offset:%3" : "=v"(bias[n]) : "v"((cg * 224 + nw * 112 + 4 * g) * 2), "s"(b1), "i"(n * 32) : "memory");
  }
  stage(0);
  stage(kSlot);
  if (FFN) asm volatile("s_waitcnt vmcnt(17)" : "+v"(srow), "+v"(ep)::"memory"); else asm volatile("s_waitcnt vmcnt(10)" : "+v"(srow), "+v"(ep)::"memory");
  tag64 = (((u64)ep[1] << 32) | ep[0]) + 1;
  const unsigned tag = (unsigned)tag64;
  {
    float v[8];
#pragma unroll
    for (int i = 0; i < 4; ++i) { v[2 * i] = blo(srow[i]); v[2 * i + 1] = bhi(srow[i]); }
    float s = ((v[0] + v[1]) + (v[2] + v[3])) + ((v[4] + v[5]) + (v[6] + v[7]));
    const float mean = wave_sum(lane < 56 ? s : 0.f) * (1.f / 448.f);
    float d = 0.f;
#pragma unroll
    for (int i = 0; i < 8; ++i) { const float e = v[i] - mean; d = fmaf(e, e, d); }
    const float var = wave_sum(lane < 56 ? d : 0.f) * (1.f / 448.f);
    const float rstd = __builtin_amdgcn_rsqf(var + 1e-5f);
    if (lane == 0) {
      const u32x4 gr = {__float_as_uint(mean), tag, __float_as_uint(rstd), tag};
      store_wt(kWsStats + strow * 16, ws, gr);
    }
  }
  unsigned ax[2], aw[4];
#pragma unroll
  for (int kk = 0; kk < 2; ++kk) ax[kk] = (32 * mw + m) * 128 + (((4 * kk + g) ^ (m >> 1)) << 4);
  {
    const int rho = 4 * (g & 1) + q + 16 * (g >> 1);
    const unsigned base = WOFF + rho * WROW + (p >> 1) * 16 + (p & 1) * 8;
#pragma unroll
    for (int i = 0; i < 4; ++i) {
      if (FFN) aw[i] = base + (((7 * nw + i) ^ (g & 1)) - i) * 32;
      else aw[i] = base + ((6 * nw + i) ^ (2 * (g & 1) + (q >> 1))) * 32;
    }
  }
  const unsigned exoff = (8 - 2 * mw) * 2048;
  f32x4 acc[2][NT] = {}, ace[NT] = {};
  bf16x8 X[2][2], W[2][NT], XE[2];
  auto rd = [&](int kk, unsigned slot) {
#pragma unroll
    for (int mt = 0; mt < 2; ++mt) X[kk][mt] = lds_b128(slot + ax[kk] + mt * 2048);
#pragma unroll
    for (int n = 0; n < NT; ++n) {
      const unsigned a = slot + (FFN ? aw[n & 1] + n * 32 : aw[n & 3] + (n >> 2) * 128) + kk * 32 * WROW;
      W[kk][n] = lds_tr(a, a + 8 * WROW);
    }
    if (!FFN) XE[kk] = lds_b128(slot + ax[kk] + exoff);
  };
  auto mm = [&](int kk) {
#pragma unroll
    for (int n = 0; n < NT; ++n)
#pragma unroll
      for (int mt = 0; mt < 2; ++mt) acc[mt][n] = mfma(W[kk][n], X[kk][mt], acc[mt][n]);
  };
  auto extra = [&](int kk) {
    const u32x4 one = {0x3f803f80u, 0x3f803f80u, 0x3f803f80u, 0x3f803f80u};
    u32x4 e = one;
    if (!FFN) { const u32x4 h = __builtin_bit_cast(u32x4, XE[kk]); e = m == 0 ? one : h; }
#pragma unroll
    for (int n = 0; n < NT; ++n) ace[n] = mfma(W[kk][n], __builtin_bit_cast(bf16x8, e), ace[n]);
  };
  const bool ex0 = mw == 2 * nw, ex1 = FFN ? mw == 2 * nw + 1 : ex0;
  u32x4 st[3];
  auto ldstats = [&]() {
#pragma unroll
    for (int mt = 0; mt < 2; ++mt)
      asm volatile("global_load_dwordx4 %0, %1, %2 offset:%3 sc1" : "=v"(st[mt]) : "v"(kWsStats + (r0 + 32 * mw + 2 * m) * 16), "s"(ws), "i"(mt * 16) : "memory");
    if (!FFN) { const int r = r0 - 16 + m; asm volatile("global_load_dwordx4 %0, %1, %2 sc1" : "=v"(st[2]) : "v"(kWsStats + (r < 0 ? 0 : r) * 16), "s"(ws) : "memory"); }
  };

  WAIT_VM(6);
  BARRIER();
  STAMP(1);
  rd(0, 0);
  stage(2 * kSlot);
  unsigned cur = 0;
#pragma nounroll
  for (int s = 0; s < 6; ++s) {
    rd(1, cur);
    mm(0);
    interleave<2 * NT, 2 + 2 * NT + (FFN ? 0 : 1)>();
    if (ex0) extra(0);
    WAIT_LGKM0();
    if (s < 5) WAIT_VM(5); else WAIT_VM(0);
    BARRIER();
    if (s < 4) stage(cur);
    cur = cur == 2 * kSlot ? 0 : cur + kSlot;
    rd(0, cur);
    mm(1);
    interleave<2 * NT, 2 + 2 * NT + (FFN ? 0 : 1)>();
    if (ex1) extra(1);
  }
  ldstats();
  rd(1, 0);
  mm(0);
  interleave<2 * NT, 2 + 2 * NT + (FFN ? 0 : 1)>();
  if (ex0) extra(0);
  WAIT_LGKM0();
  BARRIER();
#pragma unroll
  for (int i = 0; i < 4; ++i) glds(v2, w2s0 + (16 * (i >> 1) + 4 * (i & 1)) * 896, w * 12288 + i * 1024);
#pragma unroll
  for (int i = 0; i < 4; ++i) glds(v2, w2s1 + (16 * (i >> 1) + 4 * (i & 1)) * 896, w * 12288 + 4096 + i * 1024);
#pragma unroll
  for (int i = 0; i < 4; ++i) glds(v2, w2s2 + (16 * (i >> 1) + 4 * (i & 1)) * 896, w * 12288 + 8192 + i * 1024);
  mm(1);
  if (ex1) extra(1);
  STAMP(2);
  if (m == 0 && (ex0 || ex1)) {
#pragma unroll
    for (int n = 0; n < NT; ++n) *(LAS f32x4*)(size_t)(kCex + ((FFN ? (mw & 1) * 224 : 0) + (nw * NT + n) * 16 + 4 * g) * 4) = ace[n];
  }
  if (FFN) asm volatile("s_waitcnt vmcnt(12)" : "+v"(st[0]), "+v"(st[1])::"memory");
  else asm volatile("s_waitcnt vmcnt(12)" : "+v"(st[0]), "+v"(st[1]), "+v"(st[2])::"memory");
#pragma nounroll
  for (int spin = 0; spin < kSpinCap; ++spin) {
    bool ok = st[0][1] == tag && st[0][3] == tag && st[1][1] == tag && st[1][3] == tag;
    if (!FFN) ok = ok && st[2][1] == tag && st[2][3] == tag;
    if (__all(ok)) break;
    __builtin_amdgcn_s_sleep(2);
    ldstats();
    if (FFN) asm volatile("s_waitcnt vmcnt(0)" : "+v"(st[0]), "+v"(st[1])::"memory");
    else asm volatile("s_waitcnt vmcnt(0)" : "+v"(st[0]), "+v"(st[1]), "+v"(st[2])::"memory");
  }
  WAIT_LGKM0();
  BARRIER();
  const char* a2 = ws + kWsA2;
  if (FFN) {
#pragma unroll
    for (int n = 0; n < 7; ++n) asm volatile("" : "+v"(bias[n]));
    const float rs0 = __uint_as_float(st[0][2]), rs1 = __uint_as_float(st[1][2]);
    const float mu0 = -rs0 * __uint_as_float(st[0][0]), mu1 = -rs1 * __uint_as_float(st[1][0]);
    const unsigned sto = ((2 * rt + (mw >> 1)) * 72 + 7 * cg) * 4096 + (2 * (mw & 1) + (g & 1)) * 1024 + ((g >> 1) * 16 + m) * 16;
    f32x4 cs[7];
#pragma unroll
    for (int n = 0; n < 7; ++n) { const unsigned ca = kCex + ((nw * 7 + n) * 16 + 4 * g) * 4; cs[n] = lds_f4(ca) + lds_f4(ca + 896); }
#pragma unroll
    for (int n = 0; n < 7; ++n) {
      const f32x4 c = cs[n];
      const float b[4] = {blo(bias[n][0]), bhi(bias[n][0]), blo(bias[n][1]), bhi(bias[n][1])};
      float y0[4], y1[4];
#pragma unroll
      for (int r = 0; r < 4; ++r) {
        const float u0 = fmaf(rs0, acc[0][n][r], fmaf(mu0, c[r], b[r])), u1 = fmaf(rs1, acc[1][n][r], fmaf(mu1, c[r], b[r]));
        const float t0 = fmaxf(u0, 0.f), t1 = fmaxf(u1, 0.f);
        y0[r] = t0 * t0; y1[r] = t1 * t1;
      }
      const auto s0 = __builtin_amdgcn_permlane16_swap(pack2(y0[0], y0[1]), pack2(y1[0], y1[1]), false, false);
      const auto s1 = __builtin_amdgcn_permlane16_swap(pack2(y0[2], y0[3]), pack2(y1[2], y1[3]), false, false);
      const u32x4 v = {s0[0], s1[0], s0[1], s1[1]};
      const int T = 7 * nw + n;
      store_wt(sto, a2 + (T >> 1) * 4096 + (T & 1) * 512, v);
    }
  } else {
    unsigned co[4];
#pragma unroll
    for (int i = 0; i < 4; ++i) {
      const int T = 6 * nw + i, n = 16 * (T & 3) + 4 * g;
      co[i] = kAtt + (T >> 2) * 128 + (n < 28 ? n : n < 56 ? n + 4 : n < 60 ? n - 28 : n) * 2;
    }
    f32x4 cs[NT];
#pragma unroll
    for (int n = 0; n < NT; ++n) cs[n] = lds_f4(kCex + ((nw * 6 + n) * 16 + 4 * g) * 4);
    auto put = [&](const f32x4 (&a)[NT], const u32x4& s, unsigned row) {
      const float mean = __uint_as_float(s[0]), rs = __uint_as_float(s[2]);
#pragma unroll
      for (int n = 0; n < NT; ++n) {
        const f32x4 c = cs[n];
        u32x2 v = {pack2(rs * fmaf(-mean, c[0], a[n][0]), rs * fmaf(-mean, c[1], a[n][1])), pack2(rs * fmaf(-mean, c[2], a[n][2]), rs * fmaf(-mean, c[3], a[n][3]))};
        if (((6 * nw + n) & 3) == 3 && g >= 2) v = u32x2{0u, 0u};
        *(LAS u32x2*)(size_t)(co[n & 3] + (n >> 2) * 128 + row * 400) = v;
      }
    };
    put(acc[0], st[0], 8 + 16 * mw + m);
    put(acc[1], st[1], 72 + 8 + 16 * mw + m);
    if (ex0) put(ace, st[2], (m & 1) * 72 + (m >> 1));
    WAIT_LGKM0();
    BARRIER();
    const int blk = w >> 1, par = w & 1;
    const unsigned rb = kAtt + (par * 72 + 16 * blk) * 400;
    const unsigned aq = rb + (m + 8) * 400 + 16 * g, ak0 = rb + m * 400 + 128 + 16 * g, ak1 = rb + (16 + (m & 7)) * 400 + 128 + 16 * g;
    const unsigned av0 = rb + (4 * g + q) * 400 + 256 + 16 * p, av1 = rb + (16 + 4 * (g & 1) + q) * 400 + 256 + 16 * p;
    const int jmin = (16 - (r0 + 32 * blk + par) + 1) >> 1;
    const float kLog = 0.18898223650461362f * 1.4426950408889634f;
#pragma unroll
    for (int hd = 0; hd < 2; ++hd) {
      const bf16x8 qf = lds_b128(aq + hd * 64);
      const f32x4 z = {0.f, 0.f, 0.f, 0.f};
      const f32x4 s0 = mfma(lds_b128(ak0 + hd * 64), qf, z), s1 = mfma(lds_b128(ak1 + hd * 64), qf, z);
      float e[8], mx = -3.0e38f;
#pragma unroll
      for (int i = 0; i < 8; ++i) {
        const int j = 16 * (i >> 2) + 4 * g + (i & 3);
        const float v = j < jmin ? -1.0e9f * 1.4426950408889634f : (i < 4 ? s0[i & 3] : s1[i & 3]) * kLog;
        e[i] = (unsigned)(m + 8 - j) < 8u ? v : -__builtin_inff();
        mx = fmaxf(mx, e[i]);
      }
      mx = x32_max(x16_max(mx));
      float sum = 0.f;
#pragma unroll
      for (int i = 0; i < 8; ++i) { e[i] = __builtin_amdgcn_exp2f(e[i] - mx); sum += e[i]; }
      const float inv = __builtin_amdgcn_rcpf(x32_add(x16_add(sum)));
      const u32x4 pf = {pack2(e[0] * inv, e[1] * inv), pack2(e[2] * inv, e[3] * inv), pack2(e[4] * inv, e[5] * inv), pack2(e[6] * inv, e[7] * inv)};
      const f32x4 o0 = mfma(lds_tr(av0 + hd * 64, av1 + hd * 64), __builtin_bit_cast(bf16x8, pf), z);
      const f32x4 o1 = mfma(lds_tr(av0 + hd * 64 + 8, av1 + hd * 64 + 8), __builtin_bit_cast(bf16x8, pf), z);
      const u32x4 v = {pack2(o0[0], o0[1]), pack2(o0[2], o0[3]), pack2(o1[0], o1[1]), pack2(o1[2], o1[3])};
      store_wt(lane * 16, a2 + (((2 * rt + (blk >> 1)) * 72 + 56 + 2 * cg + hd) * 4 + 2 * (blk & 1) + par) * 1024, v);
    }
  }
  STAMP(3);
  asm volatile("s_waitcnt vmcnt(0) lgkmcnt(0)" ::: "memory");
  BARRIER();
  if (w == 0 && lane == 0)
    asm volatile("global_store_dwordx2 %0, %1, %2 sc1" ::"v"(kWsFlags + (rt * 16 + cg + (FFN ? 0 : 8)) * 128), "v"(tag64), "s"(ws) : "memory");
  STAMP(4);
}

__global__ __launch_bounds__(512, 2) void fused_kernel(const char* x, const char* wqkv, const char* wo, const char* w1, const char* b1, const char* w2,
                                                       const char* b2, char* out, char* ws) {
  __shared__ __attribute__((aligned(16))) char smem[kLds];
  asm volatile("" ::"v"((LAS char*)smem));
  const int lane = threadIdx.x & 63, w = __builtin_amdgcn_readfirstlane(threadIdx.x >> 6);
  const int bid = blockIdx.x, xcd = bid & 7, j = bid >> 3;
  u64 tstamp[10] = {};
  asm volatile("s_memtime %0\n\ts_waitcnt lgkmcnt(0)" : "=s"(tstamp[8])::"memory");
  STAMP(0);
  const int rt = (xcd & 1) * 8 + (j & 7), cgi = j >> 3, cg = 2 * (xcd >> 1) + (cgi & 1);
  const int k0 = (rt + 2 * cgi + (xcd >> 1)) % 7;
  const int jj = j < 28 ? j : 27, rg = 4 * xcd + jj / 7, ct = jj % 7, rot = 2 * (rg & 3);
  const int r7 = lane >> 3;
  const unsigned v2 = (8 * (r7 >> 2) + (r7 & 3)) * 896 + (((lane & 7) ^ (r7 & 6)) << 4);
  const char* w2w = w2 + w * (224 * 896) + ct * 128;
  auto hstep = [&](int i) { const int s = i + rot; return s >= 7 ? s - 7 : s; };

  u64 tag64;
  if (cgi < 2) phase1<true>(x, w1, b1, ws, tag64, lane, w, rt, cg, k0, v2, w2w + hstep(0) * (32 * 896), w2w + hstep(1) * (32 * 896), w2w + hstep(2) * (32 * 896) STAMP_PASS);
  else phase1<false>(x, wqkv, b1, ws, tag64, lane, w, rt, cg, k0, v2, w2w + hstep(0) * (32 * 896), w2w + hstep(1) * (32 * 896), w2w + hstep(2) * (32 * 896) STAMP_PASS);

  if (j >= 28) {
    if (bid == 255 && w == 0) {
#pragma nounroll
      for (int spin = 0; spin < kSpinCap; ++spin) {
        u32x2 a, b, c, d;
        asm volatile("global_load_dwordx2 %0, %4, %5 sc1\n\tglobal_load_dwordx2 %1, %4, %6 sc1\n\tglobal_load_dwordx2 %2, %4, %7 sc1\n\tglobal_load_dwordx2 %3, %4, %8 sc1\n\ts_waitcnt vmcnt(0)"
                     : "=&v"(a), "=&v"(b), "=&v"(c), "=&v"(d)
                     : "v"(lane * 128), "s"(ws + kWsFlags), "s"(ws + kWsFlags + 8192), "s"(ws + kWsFlags + 16384), "s"(ws + kWsFlags + 24576) : "memory");
        const unsigned t0 = (unsigned)tag64, t1 = (unsigned)(tag64 >> 32);
        if (__all(a[0] == t0 && b[0] == t0 && c[0] == t0 && d[0] == t0 && a[1] == t1 && b[1] == t1 && c[1] == t1 && d[1] == t1)) break;
        __builtin_amdgcn_s_sleep(2);
      }
      if (lane == 0) asm volatile("global_store_dwordx2 %0, %1, %2 sc1" ::"v"(0), "v"(tag64), "s"(ws) : "memory");
    }
    STAMP_OUT();
    return;
  }

  const int m = lane & 15, g = lane >> 4, q = m >> 2, p = lane & 3;
  unsigned aw[4];
#pragma unroll
  for (int u = 0; u < 4; ++u) aw[u] = (4 * (g & 1) + q + 16 * (g >> 1)) * 128 + ((u ^ (2 * (g & 1) + (q >> 1))) * 32) + (p >> 1) * 16 + (p & 1) * 8;
  const unsigned sA = w * 12288, sB = kAtt + w * 8192;
  bf16x8 Wf[4][4];
  auto rdw = [&](int b, unsigned slot) {
#pragma unroll
    for (int u = 0; u < 4; ++u) Wf[b][u] = lds_tr(slot + aw[u], slot + aw[u] + 1024);
  };
  auto ldw = [&](const char* src, unsigned slot, unsigned vlast) {
#pragma unroll
    for (int i = 0; i < 4; ++i) glds(i == 3 ? vlast : v2, src + (16 * (i >> 1) + 4 * (i & 1)) * 896, slot + i * 1024);
  };
  const int os = threadIdx.x >> 3, orow = rg * 64 + 32 * (os >> 5) + 2 * (os & 15) + ((os >> 4) & 1);
  const unsigned oofs = orow * 896 + ct * 128 + (threadIdx.x & 7) * 16;
  u32x4 xr, br;
  asm volatile("global_load_dwordx4 %0, %1, %2" : "=v"(xr) : "v"(oofs), "s"(x) : "memory");
  asm volatile("global_load_dwordx4 %0, %1, %2" : "=v"(br) : "v"(ct * 128 + (threadIdx.x & 7) * 16), "s"(b2) : "memory");
  rdw(0, sA); rdw(1, sA + 4096); rdw(2, sA + 8192);
  const char* wow = wo + w * (56 * 896) + ct * 128;
  const unsigned v2o = lane < 32 ? v2 : v2 - 4 * 896;
  ldw(w2w + hstep(3) * (32 * 896), sB, v2);
  ldw(w2w + hstep(4) * (32 * 896), sB + 4096, v2);
  WAIT_LGKM0();
  ldw(w2w + hstep(5) * (32 * 896), sA, v2);
  ldw(w2w + hstep(6) * (32 * 896), sA + 4096, v2);
  ldw(wow, sA + 8192, v2o);
#pragma nounroll
  for (int spin = 0; spin < kSpinCap; ++spin) {
    u32x2 f;
    asm volatile("global_load_dwordx2 %0, %1, %2 sc1\n\ts_waitcnt vmcnt(0)" : "=&v"(f) : "v"(kWsFlags + ((rg >> 1) * 16 + w + (lane & 1) * 8) * 128), "s"(ws) : "memory");
    if (__all(f[0] == (unsigned)tag64 && f[1] == (unsigned)(tag64 >> 32))) break;
    __builtin_amdgcn_s_sleep(1);
  }
  STAMP(5);
  const char* a2 = ws + kWsA2 + rg * (72 * 4096);
  u32x4 H[3][4];
  auto ldh = [&](int b, int i) {
    const char* src = a2 + (i < 7 ? 7 * w + hstep(i) : 56 + 2 * w + (i - 7)) * 4096;
#pragma unroll
    for (int mt = 0; mt < 4; ++mt) asm volatile("global_load_dwordx4 %0, %1, %2 offset:%3 sc1" : "=v"(H[b][mt]) : "v"(lane * 16), "s"(src), "i"(mt * 1024) : "memory");
  };
  ldh(0, 0); ldh(1, 1); ldh(2, 2);
  rdw(3, sB);
  WAIT_LGKM0();
  ldw(wow + 28 * 896, sB, v2o);
  f32x4 acc[4][4] = {};
#pragma unroll
  for (int i = 0; i < 9; ++i) {
    const int b = i % 3;
    if (i < 3) asm volatile("s_waitcnt vmcnt(12)" : "+v"(H[b][0]), "+v"(H[b][1]), "+v"(H[b][2]), "+v"(H[b][3])::"memory");
    else if (i < 7) asm volatile("s_waitcnt vmcnt(8)" : "+v"(H[b][0]), "+v"(H[b][1]), "+v"(H[b][2]), "+v"(H[b][3])::"memory");
    else if (i < 8) asm volatile("s_waitcnt vmcnt(4)" : "+v"(H[b][0]), "+v"(H[b][1]), "+v"(H[b][2]), "+v"(H[b][3])::"memory");
    else asm volatile("s_waitcnt vmcnt(0)" : "+v"(H[b][0]), "+v"(H[b][1]), "+v"(H[b][2]), "+v"(H[b][3]), "+v"(xr), "+v"(br)::"memory");
    if (i >= 3 && i < 8) rdw((i + 1) & 3, i == 3 ? sB + 4096 : i == 7 ? sB : sA + (i - 4) * 4096);
#pragma unroll
    for (int mt = 0; mt < 4; ++mt)
#pragma unroll
      for (int u = 0; u < 4; ++u) acc[mt][u] = mfma(Wf[i & 3][u], __builtin_bit_cast(bf16x8, H[b][mt]), acc[mt][u]);
    __builtin_amdgcn_sched_barrier(0);
    if (i + 3 < 9) ldh(b, i + 3);
  }
  STAMP(6);
  WAIT_LGKM0();
  BARRIER();
#pragma unroll
  for (int mt = 0; mt < 4; ++mt)
#pragma unroll
    for (int u = 0; u < 4; ++u) *(LAS f32x4*)(size_t)(w * 16384 + (mt * 16 + m) * 256 + (((4 * u + g) ^ (m & 7)) << 4)) = acc[mt][u];
  WAIT_LGKM0();
  BARRIER();
  f32x4 lo = {blo(xr[0]) + blo(br[0]), bhi(xr[0]) + bhi(br[0]), blo(xr[1]) + blo(br[1]), bhi(xr[1]) + bhi(br[1])};
  f32x4 hi = {blo(xr[2]) + blo(br[2]), bhi(xr[2]) + bhi(br[2]), blo(xr[3]) + blo(br[3]), bhi(xr[3]) + bhi(br[3])};
  const unsigned ra = os * 256 + (((2 * (threadIdx.x & 7)) ^ (os & 7)) << 4);
#pragma unroll
  for (int k = 0; k < 8; ++k) { lo += lds_f4(k * 16384 + ra); hi += lds_f4(k * 16384 + (ra ^ 16)); }
  const u32x4 v = {pack2(lo[0], lo[1]), pack2(lo[2], lo[3]), pack2(hi[0], hi[1]), pack2(hi[2], hi[3])};
  *(u32x4*)(out + oofs) = v;
  STAMP(7);
  STAMP_OUT();
}

extern "C" void kernel_launch(void* const* d_in, const int* in_sizes, int n_in, void* d_out, int out_size, void* d_ws, size_t ws_size, hipStream_t stream) {
  fused_kernel<<<256, 512, 0, stream>>>((const char*)d_in[0], (const char*)d_in[1], (const char*)d_in[2], (const char*)d_in[4], (const char*)d_in[5],
                                        (const char*)d_in[6], (const char*)d_in[7], (char*)d_out, (char*)d_ws);
}
```

```cpp
#include <hip/hip_runtime.h>
#include <hip/hip_bf16.h>

typedef __bf16 bf16x2 __attribute__((ext_vector_type(2)));
typedef __bf16 bf16x4 __attribute__((ext_vector_type(4)));
typedef __bf16 bf16x8 __attribute__((ext_vector_type(8)));
typedef float f32x2 __attribute__((ext_vector_type(2)));
typedef float f32x4 __attribute__((ext_vector_type(4)));
typedef unsigned u32x2 __attribute__((ext_vector_type(2)));
typedef unsigned u32x4 __attribute__((ext_vector_type(4)));
typedef unsigned long long u64;
#define LAS __attribute__((address_space(3)))
#define DEV __device__ __forceinline__

constexpr int kLds = 163840;
constexpr int kSlot = 45056;
constexpr int kAtt = 98304;
constexpr int kCex = kAtt + 144 * 400;
constexpr int kWsFlags = 4096, kWsStats = 65536, kWsA2 = 131072, kWsDiag = 16 << 20;
constexpr int kSpinCap = 1 << 16;

#define STAMP(i)
#define STAMP_ARG
#define STAMP_PASS
#define STAMP_OUT()

DEV void glds(unsigned voff, const char* sbase, unsigned dst) {
  asm volatile("s_mov_b32 m0, %2\n\ts_nop 0\n\tglobal_load_lds_dwordx4 %0, %1" ::"v"(voff), "s"(sbase), "s"(dst) : "memory");
}
DEV void store_wt(unsigned voff, const char* sbase, u32x4 v) {
  asm volatile("global_store_dwordx4 %0, %1, %2 sc1\n\ts_nop 1" ::"v"(voff), "v"(v), "s"(sbase) : "memory");
}
#define WAIT_VM(n) asm volatile("s_waitcnt vmcnt(" #n ")" ::: "memory")
#define WAIT_LGKM0() asm volatile("s_waitcnt lgkmcnt(0)" ::: "memory")
#define BARRIER() do { asm volatile("" ::: "memory"); __builtin_amdgcn_s_barrier(); asm volatile("" ::: "memory"); } while (0)

DEV bf16x8 lds_b128(unsigned a) { return *(LAS bf16x8*)(size_t)a; }
DEV f32x4 lds_f4(unsigned a) { return *(LAS f32x4*)(size_t)a; }
DEV bf16x8 lds_tr(unsigned a0, unsigned a1) {
  bf16x4 a = __builtin_amdgcn_ds_read_tr16_b64_v4bf16((LAS bf16x4*)(size_t)a0);
  bf16x4 b = __builtin_amdgcn_ds_read_tr16_b64_v4bf16((LAS bf16x4*)(size_t)a1);
  return __builtin_shufflevector(a, b, 0, 1, 2, 3, 4, 5, 6, 7);
}
DEV f32x4 mfma(bf16x8 a, bf16x8 b, f32x4 c) { return __builtin_amdgcn_mfma_f32_16x16x32_bf16(a, b, c, 0, 0, 0); }
DEV unsigned pack2(float lo, float hi) { f32x2 v = {lo, hi}; return __builtin_bit_cast(unsigned, __builtin_convertvector(v, bf16x2)); }
DEV float blo(unsigned u) { return __uint_as_float(u << 16); }
DEV float bhi(unsigned u) { return __uint_as_float(u & 0xffff0000u); }
template <int C> DEV float dpp(float x) { return __builtin_bit_cast(float, __builtin_amdgcn_update_dpp(0, __builtin_bit_cast(int, x), C, 0xF, 0xF, true)); }
DEV float x16_add(float x) { auto r = __builtin_amdgcn_permlane16_swap(__float_as_uint(x), __float_as_uint(x), false, false); return __uint_as_float(r[0]) + __uint_as_float(r[1]); }
DEV float x32_add(float x) { auto r = __builtin_amdgcn_permlane32_swap(__float_as_uint(x), __float_as_uint(x), false, false); return __uint_as_float(r[0]) + __uint_as_float(r[1]); }
DEV float x16_max(float x) { auto r = __builtin_amdgcn_permlane16_swap(__float_as_uint(x), __float_as_uint(x), false, false); return fmaxf(__uint_as_float(r[0]), __uint_as_float(r[1])); }
DEV float x32_max(float x) { auto r = __builtin_amdgcn_permlane32_swap(__float_as_uint(x), __float_as_uint(x), false, false); return fmaxf(__uint_as_float(r[0]), __uint_as_float(r[1])); }
DEV float wave_sum(float x) { x += dpp<0xB1>(x); x += dpp<0x4E>(x); x += dpp<0x141>(x); x += dpp<0x140>(x); return x32_add(x16_add(x)); }
DEV int swap23(int r) { return (r & ~12) | ((r & 4) << 1) | ((r & 8) >> 1); }

template <int NM, int NR> DEV void interleave() {
#pragma unroll
  for (int q = 0; q < NM; ++q) {
    __builtin_amdgcn_sched_group_barrier(0x8, 1, 0);
    if (q * 2 + 1 < NR) __builtin_amdgcn_sched_group_barrier(0x100, 2, 0);
    else if (q * 2 < NR) __builtin_amdgcn_sched_group_barrier(0x100, 1, 0);
  }
}

template <bool FFN>
DEV void phase1(const char* x, const char* wmat, const char* b1, char* ws, u64& tag64, int lane, int w, int rt, int cg, int k0,
                unsigned v2, const char* w2s0, const char* w2s1, const char* w2s2 STAMP_ARG) {
  constexpr int NT = FFN ? 7 : 6;
  constexpr int WOFF = FFN ? 16384 : 18432, WROW = FFN ? 448 : 384, CPR = FFN ? 28 : 24, GROW = FFN ? 3584 : 2688;
  const int mw = w & 3, nw = w >> 2, m = lane & 15, g = lane >> 4, q = m >> 2, p = lane & 3;
  const int r0 = rt * 128;
  u32x2 ep;
  asm volatile("global_load_dwordx2 %0, %1, %2 sc1" : "=v"(ep) : "v"(0), "s"(ws) : "memory");
  unsigned xo[2], wo[3], oo;
#pragma unroll
  for (int u = 0; u < 2; ++u) {
    const int sg = (2 * w + u) * 8 + (lane >> 3), t = sg >> 4;
    xo[u] = (r0 + 32 * (t >> 1) + 2 * (sg & 15) + (t & 1)) * 896 + (((lane & 7) ^ ((sg >> 1) & 7)) << 4);
  }
  auto woff = [&](int i) -> unsigned {
    const int L = i * 64 + lane, rho = L / CPR, c1 = L - rho * CPR, k = swap23(rho);
    if (FFN) return k * GROW + cg * 448 + ((c1 ^ ((rho >> 1) & 2)) << 4);
    const int c = c1 ^ (rho & 6), cc = (c & 7) < 6 ? (c & 7) : 6;
    return k * GROW + (c >> 3) * 896 + cg * 112 + (cc << 4);
  };
#pragma unroll
  for (int u = 0; u < 3; ++u) wo[u] = woff(3 * w + u);
  if (FFN) oo = woff(24 + (w & 3));
  else { const int sg = 128 + (w & 1) * 8 + (lane >> 3); int r = r0 - 16 + (sg & 15); r = r < 0 ? 0 : r; oo = r * 896 + (((lane & 7) ^ ((sg >> 1) & 7)) << 4); }
  const bool opt = FFN ? w < 4 : w < 2;
  int ks = k0;
  auto stage = [&](unsigned slot) {
    const char* xb = x + ks * 128;
    const char* wb = wmat + ks * (64 * GROW);
    glds(xo[0], xb, slot + w * 2048);
    glds(wo[0], wb, slot + WOFF + w * 3072);
    glds(xo[1], xb, slot + w * 2048 + 1024);
    glds(wo[1], wb, slot + WOFF + w * 3072 + 1024);
    glds(wo[2], wb, slot + WOFF + w * 3072 + 2048);
    if (opt) glds(oo, FFN ? wb : xb, FFN ? slot + WOFF + (24 + w) * 1024 : slot + (16 + w) * 1024);
    ks = ks == 6 ? 0 : ks + 1;
  };
  u32x2 bias[7];
  u32x4 srow;
  const int strow = blockIdx.x * 8 + w;
  asm volatile("global_load_dwordx4 %0, %1, %2" : "=v"(srow) : "v"(strow * 896 + (lane < 56 ? lane : 55) * 16), "s"(x) : "memory");
  if (FFN) {
#pragma unroll
    for (int n = 0; n < 7; ++n)
      asm volatile("global_load_dwordx2 %0, %1, %2 offset:%3" : "=v"(bias[n]) : "v"((cg * 224 + nw * 112 + 4 * g) * 2), "s"(b1), "i"(n * 32) : "memory");
  }
  stage(0);
  stage(kSlot);
  if (FFN) asm volatile("s_waitcnt vmcnt(17)" : "+v"(srow), "+v"(ep)::"memory"); else asm volatile("s_waitcnt vmcnt(10)" : "+v"(srow), "+v"(ep)::"memory");
  tag64 = (((u64)ep[1] << 32) | ep[0]) + 1;
  const unsigned tag = (unsigned)tag64;
  {
    float v[8];
#pragma unroll
    for (int i = 0; i < 4; ++i) { v[2 * i] = blo(srow[i]); v[2 * i + 1] = bhi(srow[i]); }
    float s = ((v[0] + v[1]) + (v[2] + v[3])) + ((v[4] + v[5]) + (v[6] + v[7]));
    const float mean = wave_sum(lane < 56 ? s : 0.f) * (1.f / 448.f);
    float d = 0.f;
#pragma unroll
    for (int i = 0; i < 8; ++i) { const float e = v[i] - mean; d = fmaf(e, e, d); }
    const float var = wave_sum(lane < 56 ? d : 0.f) * (1.f / 448.f);
    const float rstd = __builtin_amdgcn_rsqf(var + 1e-5f);
    if (lane == 0) {
      const u32x4 gr = {__float_as_uint(mean), tag, __float_as_uint(rstd), tag};
      store_wt(kWsStats + strow * 16, ws, gr);
    }
  }
  unsigned ax[2], aw[4];
#pragma unroll
  for (int kk = 0; kk < 2; ++kk) ax[kk] = (32 * mw + m) * 128 + (((4 * kk + g) ^ (m >> 1)) << 4);
  {
    const int rho = 4 * (g & 1) + q + 16 * (g >> 1);
    const unsigned base = WOFF + rho * WROW + (p >> 1) * 16 + (p & 1) * 8;
#pragma unroll
    for (int i = 0; i < 4; ++i) {
      if (FFN) aw[i] = base + (((7 * nw + i) ^ (g & 1)) - i) * 32;
      else aw[i] = base + ((6 * nw + i) ^ (2 * (g & 1) + (q >> 1))) * 32;
    }
  }
  const unsigned exoff = (8 - 2 * mw) * 2048;
  f32x4 acc[2][NT] = {}, ace[NT] = {};
  bf16x8 X[2][2], W[2][NT], XE[2];
  auto rd = [&](int kk, unsigned slot) {
#pragma unroll
    for (int mt = 0; mt < 2; ++mt) X[kk][mt] = lds_b128(slot + ax[kk] + mt * 2048);
#pragma unroll
    for (int n = 0; n < NT; ++n) {
      const unsigned a = slot + (FFN ? aw[n & 1] + n * 32 : aw[n & 3] + (n >> 2) * 128) + kk * 32 * WROW;
      W[kk][n] = lds_tr(a, a + 8 * WROW);
    }
    if (!FFN) XE[kk] = lds_b128(slot + ax[kk] + exoff);
  };
  auto mm = [&](int kk) {
#pragma unroll
    for (int n = 0; n < NT; ++n)
#pragma unroll
      for (int mt = 0; mt < 2; ++mt) acc[mt][n] = mfma(W[kk][n], X[kk][mt], acc[mt][n]);
  };
  auto extra = [&](int kk) {
    const u32x4 one = {0x3f803f80u, 0x3f803f80u, 0x3f803f80u, 0x3f803f80u};
    u32x4 e = one;
    if (!FFN) { const u32x4 h = __builtin_bit_cast(u32x4, XE[kk]); e = m == 0 ? one : h; }
#pragma unroll
    for (int n = 0; n < NT; ++n) ace[n] = mfma(W[kk][n], __builtin_bit_cast(bf16x8, e), ace[n]);
  };
  const bool ex0 = mw == 2 * nw, ex1 = FFN ? mw == 2 * nw + 1 : ex0;
  u32x4 st[3];
  auto ldstats = [&]() {
#pragma unroll
    for (int mt = 0; mt < 2; ++mt)
      asm volatile("global_load_dwordx4 %0, %1, %2 offset:%3 sc1" : "=v"(st[mt]) : "v"(kWsStats + (r0 + 32 * mw + 2 * m) * 16), "s"(ws), "i"(mt * 16) : "memory");
    if (!FFN) { const int r = r0 - 16 + m; asm volatile("global_load_dwordx4 %0, %1, %2 sc1" : "=v"(st[2]) : "v"(kWsStats + (r < 0 ? 0 : r) * 16), "s"(ws) : "memory"); }
  };

  WAIT_VM(6);
  BARRIER();
  STAMP(1);
  rd(0, 0);
  stage(2 * kSlot);
  unsigned cur = 0;
#pragma nounroll
  for (int s = 0; s < 6; ++s) {
    rd(1, cur);
    mm(0);
    interleave<2 * NT, 2 + 2 * NT + (FFN ? 0 : 1)>();
    if (ex0) extra(0);
    WAIT_LGKM0();
    if (s < 5) WAIT_VM(5); else WAIT_VM(0);
    BARRIER();
    if (s < 4) stage(cur);
    cur = cur == 2 * kSlot ? 0 : cur + kSlot;
    rd(0, cur);
    mm(1);
    interleave<2 * NT, 2 + 2 * NT + (FFN ? 0 : 1)>();
    if (ex1) extra(1);
  }
  ldstats();
  rd(1, 0);
  mm(0);
  interleave<2 * NT, 2 + 2 * NT + (FFN ? 0 : 1)>();
  if (ex0) extra(0);
  WAIT_LGKM0();
  BARRIER();
#pragma unroll
  for (int i = 0; i < 4; ++i) glds(v2, w2s0 + (16 * (i >> 1) + 4 * (i & 1)) * 896, w * 12288 + i * 1024);
#pragma unroll
  for (int i = 0; i < 4; ++i) glds(v2, w2s1 + (16 * (i >> 1) + 4 * (i & 1)) * 896, w * 12288 + 4096 + i * 1024);
#pragma unroll
  for (int i = 0; i < 4; ++i) glds(v2, w2s2 + (16 * (i >> 1) + 4 * (i & 1)) * 896, w * 12288 + 8192 + i * 1024);
  mm(1);
  if (ex1) extra(1);
  STAMP(2);
  if (m == 0 && (ex0 || ex1)) {
#pragma unroll
    for (int n = 0; n < NT; ++n) *(LAS f32x4*)(size_t)(kCex + ((FFN ? (mw & 1) * 224 : 0) + (nw * NT + n) * 16 + 4 * g) * 4) = ace[n];
  }
  if (FFN) asm volatile("s_waitcnt vmcnt(12)" : "+v"(st[0]), "+v"(st[1])::"memory");
  else asm volatile("s_waitcnt vmcnt(12)" : "+v"(st[0]), "+v"(st[1]), "+v"(st[2])::"memory");
#pragma nounroll
  for (int spin = 0; spin < kSpinCap; ++spin) {
    bool ok = st[0][1] == tag && st[0][3] == tag && st[1][1] == tag && st[1][3] == tag;
    if (!FFN) ok = ok && st[2][1] == tag && st[2][3] == tag;
    if (__all(ok)) break;
    __builtin_amdgcn_s_sleep(2);
    ldstats();
    if (FFN) asm volatile("s_waitcnt vmcnt(0)" : "+v"(st[0]), "+v"(st[1])::"memory");
    else asm volatile("s_waitcnt vmcnt(0)" : "+v"(st[0]), "+v"(st[1]), "+v"(st[2])::"memory");
  }
  WAIT_LGKM0();
  BARRIER();
  const char* a2 = ws + kWsA2;
  if (FFN) {
#pragma unroll
    for (int n = 0; n < 7; ++n) asm volatile("" : "+v"(bias[n]));
    const float rs0 = __uint_as_float(st[0][2]), rs1 = __uint_as_float(st[1][2]);
    const float mu0 = -rs0 * __uint_as_float(st[0][0]), mu1 = -rs1 * __uint_as_float(st[1][0]);
    const unsigned sto = ((2 * rt + (mw >> 1)) * 72 + 7 * cg) * 4096 + (2 * (mw & 1) + (g & 1)) * 1024 + ((g >> 1) * 16 + m) * 16;
    f32x4 cs[7];
#pragma unroll
    for (int n = 0; n < 7; ++n) { const unsigned ca = kCex + ((nw * 7 + n) * 16 + 4 * g) * 4; cs[n] = lds_f4(ca) + lds_f4(ca + 896); }
#pragma unroll
    for (int n = 0; n < 7; ++n) {
      const f32x4 c = cs[n];
      const float b[4] = {blo(bias[n][0]), bhi(bias[n][0]), blo(bias[n][1]), bhi(bias[n][1])};
      float y0[4], y1[4];
#pragma unroll
      for (int r = 0; r < 4; ++r) {
        const float u0 = fmaf(rs0, acc[0][n][r], fmaf(mu0, c[r], b[r])), u1 = fmaf(rs1, acc[1][n][r], fmaf(mu1, c[r], b[r]));
        const float t0 = fmaxf(u0, 0.f), t1 = fmaxf(u1, 0.f);
        y0[r] = t0 * t0; y1[r] = t1 * t1;
      }
      const auto s0 = __builtin_amdgcn_permlane16_swap(pack2(y0[0], y0[1]), pack2(y1[0], y1[1]), false, false);
      const auto s1 = __builtin_amdgcn_permlane16_swap(pack2(y0[2], y0[3]), pack2(y1[2], y1[3]), false, false);
      const u32x4 v = {s0[0], s1[0], s0[1], s1[1]};
      const int T = 7 * nw + n;
      store_wt(sto, a2 + (T >> 1) * 4096 + (T & 1) * 512, v);
    }
  } else {
    unsigned co[4];
#pragma unroll
    for (int i = 0; i < 4; ++i) {
      const int T = 6 * nw + i, n = 16 * (T & 3) + 4 * g;
      co[i] = kAtt + (T >> 2) * 128 + (n < 28 ? n : n < 56 ? n + 4 : n < 60 ? n - 28 : n) * 2;
    }
    f32x4 cs[NT];
#pragma unroll
    for (int n = 0; n < NT; ++n) cs[n] = lds_f4(kCex + ((nw * 6 + n) * 16 + 4 * g) * 4);
    auto put = [&](const f32x4 (&a)[NT], const u32x4& s, unsigned row) {
      const float mean = __uint_as_float(s[0]), rs = __uint_as_float(s[2]);
#pragma unroll
      for (int n = 0; n < NT; ++n) {
        const f32x4 c = cs[n];
        u32x2 v = {pack2(rs * fmaf(-mean, c[0], a[n][0]), rs * fmaf(-mean, c[1], a[n][1])), pack2(rs * fmaf(-mean, c[2], a[n][2]), rs * fmaf(-mean, c[3], a[n][3]))};
        if (((6 * nw + n) & 3) == 3 && g >= 2) v = u32x2{0u, 0u};
        *(LAS u32x2*)(size_t)(co[n & 3] + (n >> 2) * 128 + row * 400) = v;
      }
    };
    put(acc[0], st[0], 8 + 16 * mw + m);
    put(acc[1], st[1], 72 + 8 + 16 * mw + m);
    if (ex0) put(ace, st[2], (m & 1) * 72 + (m >> 1));
    WAIT_LGKM0();
    BARRIER();
    const int blk = w >> 1, par = w & 1;
    const unsigned rb = kAtt + (par * 72 + 16 * blk) * 400;
    const unsigned aq = rb + (m + 8) * 400 + 16 * g, ak0 = rb + m * 400 + 128 + 16 * g, ak1 = rb + (16 + (m & 7)) * 400 + 128 + 16 * g;
    const unsigned av0 = rb + (4 * g + q) * 400 + 256 + 16 * p, av1 = rb + (16 + 4 * (g & 1) + q) * 400 + 256 + 16 * p;
    const int jmin = (16 - (r0 + 32 * blk + par) + 1) >> 1;
    const float kLog = 0.18898223650461362f * 1.4426950408889634f;
#pragma unroll
    for (int hd = 0; hd < 2; ++hd) {
      const bf16x8 qf = lds_b128(aq + hd * 64);
      const f32x4 z = {0.f, 0.f, 0.f, 0.f};
      const f32x4 s0 = mfma(lds_b128(ak0 + hd * 64), qf, z), s1 = mfma(lds_b128(ak1 + hd * 64), qf, z);
      float e[8], mx = -3.0e38f;
#pragma unroll
      for (int i = 0; i < 8; ++i) {
        const int j = 16 * (i >> 2) + 4 * g + (i & 3);
        const float v = j < jmin ? -1.0e9f * 1.4426950408889634f : (i < 4 ? s0[i & 3] : s1[i & 3]) * kLog;
        e[i] = (unsigned)(m + 8 - j) < 8u ? v : -__builtin_inff();
        mx = fmaxf(mx, e[i]);
      }
      mx = x32_max(x16_max(mx));
      float sum = 0.f;
#pragma unroll
      for (int i = 0; i < 8; ++i) { e[i] = __builtin_amdgcn_exp2f(e[i] - mx); sum += e[i]; }
      const float inv = __builtin_amdgcn_rcpf(x32_add(x16_add(sum)));
      const u32x4 pf = {pack2(e[0] * inv, e[1] * inv), pack2(e[2] * inv, e[3] * inv), pack2(e[4] * inv, e[5] * inv), pack2(e[6] * inv, e[7] * inv)};
      const f32x4 o0 = mfma(lds_tr(av0 + hd * 64, av1 + hd * 64), __builtin_bit_cast(bf16x8, pf), z);
      const f32x4 o1 = mfma(lds_tr(av0 + hd * 64 + 8, av1 + hd * 64 + 8), __builtin_bit_cast(bf16x8, pf), z);
      const u32x4 v = {pack2(o0[0], o0[1]), pack2(o0[2], o0[3]), pack2(o1[0], o1[1]), pack2(o1[2], o1[3])};
      store_wt(lane * 16, a2 + (((2 * rt + (blk >> 1)) * 72 + 56 + 2 * cg + hd) * 4 + 2 * (blk & 1) + par) * 1024, v);
    }
  }
  STAMP(3);
  asm volatile("s_waitcnt vmcnt(0) lgkmcnt(0)" ::: "memory");
  BARRIER();
  if (w == 0 && lane == 0)
    asm volatile("global_store_dwordx2 %0, %1, %2 sc1" ::"v"(kWsFlags + (rt * 16 + cg + (FFN ? 0 : 8)) * 128), "v"(tag64), "s"(ws) : "memory");
  STAMP(4);
}

__global__ __launch_bounds__(512, 2) void fused_kernel(const char* x, const char* wqkv, const char* wo, const char* w1, const char* b1, const char* w2,
                                                       const char* b2, char* out, char* ws) {
  __shared__ __attribute__((aligned(16))) char smem[kLds];
  asm volatile("" ::"v"((LAS char*)smem));
  const int lane = threadIdx.x & 63, w = __builtin_amdgcn_readfirstlane(threadIdx.x >> 6);
  const int bid = blockIdx.x, xcd = bid & 7, j = bid >> 3;
  STAMP(0);
  const int rt = (xcd & 1) * 8 + (j & 7), cgi = j >> 3, cg = 2 * (xcd >> 1) + (cgi & 1);
  const int k0 = (rt + 2 * cgi + (xcd >> 1)) % 7;
  const int jj = j < 28 ? j : 27, rg = 4 * xcd + jj / 7, ct = jj % 7, rot = 2 * (rg & 3);
  const int r7 = lane >> 3;
  const unsigned v2 = (8 * (r7 >> 2) + (r7 & 3)) * 896 + (((lane & 7) ^ (r7 & 6)) << 4);
  const char* w2w = w2 + w * (224 * 896) + ct * 128;
  auto hstep = [&](int i) { const int s = i + rot; return s >= 7 ? s - 7 : s; };

  u64 tag64;
  if (cgi < 2) phase1<true>(x, w1, b1, ws, tag64, lane, w, rt, cg, k0, v2, w2w + hstep(0) * (32 * 896), w2w + hstep(1) * (32 * 896), w2w + hstep(2) * (32 * 896) STAMP_PASS);
  else phase1<false>(x, wqkv, b1, ws, tag64, lane, w, rt, cg, k0, v2, w2w + hstep(0) * (32 * 896), w2w + hstep(1) * (32 * 896), w2w + hstep(2) * (32 * 896) STAMP_PASS);

  if (j >= 28) {
    if (bid == 255 && w == 0) {
#pragma nounroll
      for (int spin = 0; spin < kSpinCap; ++spin) {
        u32x2 a, b, c, d;
        asm volatile("global_load_dwordx2 %0, %4, %5 sc1\n\tglobal_load_dwordx2 %1, %4, %6 sc1\n\tglobal_load_dwordx2 %2, %4, %7 sc1\n\tglobal_load_dwordx2 %3, %4, %8 sc1\n\ts_waitcnt vmcnt(0)"
                     : "=&v"(a), "=&v"(b), "=&v"(c), "=&v"(d)
                     : "v"(lane * 128), "s"(ws + kWsFlags), "s"(ws + kWsFlags + 8192), "s"(ws + kWsFlags + 16384), "s"(ws + kWsFlags + 24576) : "memory");
        const unsigned t0 = (unsigned)tag64, t1 = (unsigned)(tag64 >> 32);
        if (__all(a[0] == t0 && b[0] == t0 && c[0] == t0 && d[0] == t0 && a[1] == t1 && b[1] == t1 && c[1] == t1 && d[1] == t1)) break;
        __builtin_amdgcn_s_sleep(2);
      }
      if (lane == 0) asm volatile("global_store_dwordx2 %0, %1, %2 sc1" ::"v"(0), "v"(tag64), "s"(ws) : "memory");
    }
    STAMP_OUT();
    return;
  }

  const int m = lane & 15, g = lane >> 4, q = m >> 2, p = lane & 3;
  unsigned aw[4];
#pragma unroll
  for (int u = 0; u < 4; ++u) aw[u] = (4 * (g & 1) + q + 16 * (g >> 1)) * 128 + ((u ^ (2 * (g & 1) + (q >> 1))) * 32) + (p >> 1) * 16 + (p & 1) * 8;
  const unsigned sA = w * 12288, sB = kAtt + w * 8192;
  bf16x8 Wf[4][4];
  auto rdw = [&](int b, unsigned slot) {
#pragma unroll
    for (int u = 0; u < 4; ++u) Wf[b][u] = lds_tr(slot + aw[u], slot + aw[u] + 1024);
  };
  auto ldw = [&](const char* src, unsigned slot, unsigned vlast) {
#pragma unroll
    for (int i = 0; i < 4; ++i) glds(i == 3 ? vlast : v2, src + (16 * (i >> 1) + 4 * (i & 1)) * 896, slot + i * 1024);
  };
  const int os = threadIdx.x >> 3, orow = rg * 64 + 32 * (os >> 5) + 2 * (os & 15) + ((os >> 4) & 1);
  const unsigned oofs = orow * 896 + ct * 128 + (threadIdx.x & 7) * 16;
  u32x4 xr, br;
  asm volatile("global_load_dwordx4 %0, %1, %2" : "=v"(xr) : "v"(oofs), "s"(x) : "memory");
  asm volatile("global_load_dwordx4 %0, %1, %2" : "=v"(br) : "v"(ct * 128 + (threadIdx.x & 7) * 16), "s"(b2) : "memory");
  rdw(0, sA); rdw(1, sA + 4096); rdw(2, sA + 8192);
  const char* wow = wo + w * (56 * 896) + ct * 128;
  const unsigned v2o = lane < 32 ? v2 : v2 - 4 * 896;
  ldw(w2w + hstep(3) * (32 * 896), sB, v2);
  ldw(w2w + hstep(4) * (32 * 896), sB + 4096, v2);
  WAIT_LGKM0();
  ldw(w2w + hstep(5) * (32 * 896), sA, v2);
  ldw(w2w + hstep(6) * (32 * 896), sA + 4096, v2);
  ldw(wow, sA + 8192, v2o);
#pragma nounroll
  for (int spin = 0; spin < kSpinCap; ++spin) {
    u32x2 f;
    asm volatile("global_load_dwordx2 %0, %1, %2 sc1\n\ts_waitcnt vmcnt(0)" : "=&v"(f) : "v"(kWsFlags + ((rg >> 1) * 16 + w + (lane & 1) * 8) * 128), "s"(ws) : "memory");
    if (__all(f[0] == (unsigned)tag64 && f[1] == (unsigned)(tag64 >> 32))) break;
    __builtin_amdgcn_s_sleep(1);
  }
  STAMP(5);
  const char* a2 = ws + kWsA2 + rg * (72 * 4096);
  u32x4 H[3][4];
  auto ldh = [&](int b, int i) {
    const char* src = a2 + (i < 7 ? 7 * w + hstep(i) : 56 + 2 * w + (i - 7)) * 4096;
#pragma unroll
    for (int mt = 0; mt < 4; ++mt) asm volatile("global_load_dwordx4 %0, %1, %2 offset:%3 sc1" : "=v"(H[b][mt]) : "v"(lane * 16), "s"(src), "i"(mt * 1024) : "memory");
  };
  ldh(0, 0); ldh(1, 1); ldh(2, 2);
  rdw(3, sB);
  WAIT_LGKM0();
  ldw(wow + 28 * 896, sB, v2o);
  f32x4 acc[4][4] = {};
#pragma unroll
  for (int i = 0; i < 9; ++i) {
    const int b = i % 3;
    if (i < 3) asm volatile("s_waitcnt vmcnt(12)" : "+v"(H[b][0]), "+v"(H[b][1]), "+v"(H[b][2]), "+v"(H[b][3])::"memory");
    else if (i < 7) asm volatile("s_waitcnt vmcnt(8)" : "+v"(H[b][0]), "+v"(H[b][1]), "+v"(H[b][2]), "+v"(H[b][3])::"memory");
    else if (i < 8) asm volatile("s_waitcnt vmcnt(4)" : "+v"(H[b][0]), "+v"(H[b][1]), "+v"(H[b][2]), "+v"(H[b][3])::"memory");
    else asm volatile("s_waitcnt vmcnt(0)" : "+v"(H[b][0]), "+v"(H[b][1]), "+v"(H[b][2]), "+v"(H[b][3]), "+v"(xr), "+v"(br)::"memory");
    if (i >= 3 && i < 8) rdw((i + 1) & 3, i == 3 ? sB + 4096 : i == 7 ? sB : sA + (i - 4) * 4096);
#pragma unroll
    for (int mt = 0; mt < 4; ++mt)
#pragma unroll
      for (int u = 0; u < 4; ++u) acc[mt][u] = mfma(Wf[i & 3][u], __builtin_bit_cast(bf16x8, H[b][mt]), acc[mt][u]);
    __builtin_amdgcn_sched_barrier(0);
    if (i + 3 < 9) ldh(b, i + 3);
  }
  STAMP(6);
  WAIT_LGKM0();
  BARRIER();
#pragma unroll
  for (int mt = 0; mt < 4; ++mt)
#pragma unroll
    for (int u = 0; u < 4; ++u) *(LAS f32x4*)(size_t)(w * 16384 + (mt * 16 + m) * 256 + (((4 * u + g) ^ (m & 7)) << 4)) = acc[mt][u];
  WAIT_LGKM0();
  BARRIER();
  f32x4 lo = {blo(xr[0]) + blo(br[0]), bhi(xr[0]) + bhi(br[0]), blo(xr[1]) + blo(br[1]), bhi(xr[1]) + bhi(br[1])};
  f32x4 hi = {blo(xr[2]) + blo(br[2]), bhi(xr[2]) + bhi(br[2]), blo(xr[3]) + blo(br[3]), bhi(xr[3]) + bhi(br[3])};
  const unsigned ra = os * 256 + (((2 * (threadIdx.x & 7)) ^ (os & 7)) << 4);
#pragma unroll
  for (int k = 0; k < 8; ++k) { lo += lds_f4(k * 16384 + ra); hi += lds_f4(k * 16384 + (ra ^ 16)); }
  const u32x4 v = {pack2(lo[0], lo[1]), pack2(lo[2], lo[3]), pack2(hi[0], hi[1]), pack2(hi[2], hi[3])};
  *(u32x4*)(out + oofs) = v;
  STAMP(7);
  STAMP_OUT();
}

__global__ void k_pre(char* ws) { u64 t = __builtin_amdgcn_s_memrealtime(); if (threadIdx.x == 0) *(u64*)(ws + kWsDiag) = t; }
__global__ void k_post(char* ws) { u64 t = *(volatile u64*)(ws + kWsDiag); u64 n; do { __builtin_amdgcn_s_sleep(4); n = __builtin_amdgcn_s_memrealtime(); } while (n < t + 6000 && n + 100000 > t); }
extern "C" void kernel_launch(void* const* d_in, const int* in_sizes, int n_in, void* d_out, int out_size, void* d_ws, size_t ws_size, hipStream_t stream) {
  fused_kernel<<<256, 512, 0, stream>>>((const char*)d_in[0], (const char*)d_in[1], (const char*)d_in[2], (const char*)d_in[4], (const char*)d_in[5],
                                        (const char*)d_in[6], (const char*)d_in[7], (char*)d_out, (char*)d_ws);
}
```

```cpp
#include <hip/hip_runtime.h>
#include <hip/hip_bf16.h>
#define TOUCH_S 2
#define MAP2 1
#define W4N 4
#define ROT 6
#define S1LATE 0

typedef __bf16 bf16x2 __attribute__((ext_vector_type(2)));
typedef __bf16 bf16x4 __attribute__((ext_vector_type(4)));
typedef __bf16 bf16x8 __attribute__((ext_vector_type(8)));
typedef float f32x2 __attribute__((ext_vector_type(2)));
typedef float f32x4 __attribute__((ext_vector_type(4)));
typedef unsigned u32x2 __attribute__((ext_vector_type(2)));
typedef unsigned u32x4 __attribute__((ext_vector_type(4)));
typedef unsigned u32x16 __attribute__((ext_vector_type(16)));
typedef unsigned long long u64;
#define LAS __attribute__((address_space(3)))
#define DEV __device__ __forceinline__

constexpr int kLds = 163840;
constexpr int kSlot = 45056;
constexpr int kCex = kLds - 1024;
constexpr int kAtt = kCex - 144 * 400;
constexpr int kScr = 3 * kSlot;
constexpr int kWsFlags = 4096, kWsStats = 131072, kWsA2 = 262144, kWsDiag = 16 << 20;
constexpr int kSpinCap = 1 << 16;

#define STAMP(i)
#define STAMP_ARG
#define STAMP_PASS
#define STAMP_INIT()
#define STAMP_OUT()
#define CSTAMP(i)
#define CS(set, i)

DEV void set_m0(unsigned a, unsigned b) {
  if (__builtin_constant_p(a)) asm volatile("s_add_u32 m0, %1, %0" ::"i"(a), "s"(b) : "scc");
  else asm volatile("s_add_u32 m0, %0, %1" ::"s"(a), "s"(b) : "scc");
}
DEV void glds_m0(unsigned voff, const char* sbase) { asm volatile("global_load_lds_dwordx4 %0, %1" ::"v"(voff), "s"(sbase) : "memory"); }
template <int LANES> DEV void glds_part_m0(unsigned voff, const char* sbase) {
  asm volatile("s_mov_b64 exec, %2\n\tglobal_load_lds_dwordx4 %0, %1\n\ts_mov_b64 exec, -1" ::"v"(voff), "s"(sbase), "s"((1ull << LANES) - 1) : "memory");
}
template <int LANES> DEV void glds_dev(unsigned voff, const char* sbase, unsigned dst) {
  asm volatile("s_mov_b32 m0, %2\n\ts_mov_b64 exec, %3\n\tglobal_load_lds_dwordx4 %0, %1 sc1\n\ts_mov_b64 exec, -1" ::"v"(voff), "s"(sbase), "s"(dst), "s"(LANES == 64 ? ~0ull : (1ull << (LANES & 63)) - 1) : "memory");
}
DEV void touch(unsigned voff, const char* sbase, unsigned dst, u64 lanes) {
  asm volatile("s_mov_b32 m0, %2\n\ts_mov_b64 exec, %3\n\tglobal_load_lds_dword %0, %1\n\ts_mov_b64 exec, -1" ::"v"(voff), "s"(sbase), "s"(dst), "s"(lanes) : "memory");
}
DEV void glds(unsigned voff, const char* sbase, unsigned dst) {
  asm volatile("s_mov_b32 m0, %2\n\ts_nop 0\n\tglobal_load_lds_dwordx4 %0, %1" ::"v"(voff), "s"(sbase), "s"(dst) : "memory");
}
DEV void store_wt_lane0(unsigned voff, const char* sbase, u32x4 v) {
  asm volatile("s_mov_b64 exec, 1\n\tglobal_store_dwordx4 %0, %1, %2 sc1\n\ts_mov_b64 exec, -1\n\ts_nop 0" ::"v"(voff), "v"(v), "s"(sbase) : "memory");
}
DEV void store_flag(unsigned voff, const char* sbase, u64 v) {
  asm volatile("s_mov_b64 exec, 1\n\tglobal_store_dwordx2 %0, %1, %2 sc1\n\ts_mov_b64 exec, -1" ::"v"(voff), "v"(v), "s"(sbase) : "memory");
}
DEV void store_wt(unsigned voff, const char* sbase, u32x4 v) {
  asm volatile("global_store_dwordx4 %0, %1, %2 sc1\n\ts_nop 1" ::"v"(voff), "v"(v), "s"(sbase) : "memory");
}
#define PIN() __builtin_amdgcn_sched_barrier(0)
#define OPAQUE(x) asm volatile("" : "+v"(x))
#define WAIT_VM(n) asm volatile("s_waitcnt vmcnt(%0)" ::"i"(n) : "memory")
#define WAIT_LGKM0() do { asm volatile("" ::: "memory"); __builtin_amdgcn_s_waitcnt(0xC07F); asm volatile("" ::: "memory"); } while (0)
#define BARRIER() do { asm volatile("" ::: "memory"); __builtin_amdgcn_s_barrier(); asm volatile("" ::: "memory"); } while (0)

DEV bf16x8 lds_b128(unsigned a) { return *(LAS bf16x8*)(size_t)a; }
DEV f32x4 lds_f4(unsigned a) { return *(LAS f32x4*)(size_t)a; }
DEV bf16x8 lds_tr(unsigned a0, unsigned a1) {
  bf16x4 a = __builtin_amdgcn_ds_read_tr16_b64_v4bf16((LAS bf16x4*)(size_t)a0);
  bf16x4 b = __builtin_amdgcn_ds_read_tr16_b64_v4bf16((LAS bf16x4*)(size_t)a1);
  return __builtin_shufflevector(a, b, 0, 1, 2, 3, 4, 5, 6, 7);
}
DEV f32x4 mfma(bf16x8 a, bf16x8 b, f32x4 c) { return __builtin_amdgcn_mfma_f32_16x16x32_bf16(a, b, c, 0, 0, 0); }
DEV unsigned pack2(float lo, float hi) { f32x2 v = {lo, hi}; return __builtin_bit_cast(unsigned, __builtin_convertvector(v, bf16x2)); }
DEV unsigned pack2(f32x2 v) { return __builtin_bit_cast(unsigned, __builtin_convertvector(v, bf16x2)); }
DEV float blo(unsigned u) { return __uint_as_float(u << 16); }
DEV float bhi(unsigned u) { return __uint_as_float(u & 0xffff0000u); }
DEV f32x2 lo2(f32x4 v) { return __builtin_shufflevector(v, v, 0, 1); }
DEV f32x2 hi2(f32x4 v) { return __builtin_shufflevector(v, v, 2, 3); }
DEV f32x2 fma2(float a, f32x2 b, f32x2 c) { return __builtin_elementwise_fma(f32x2{a, a}, b, c); }
template <int C> DEV float dpp(float x) { return __builtin_bit_cast(float, __builtin_amdgcn_update_dpp(0, __builtin_bit_cast(int, x), C, 0xF, 0xF, true)); }
DEV float x16_add(float x) { auto r = __builtin_amdgcn_permlane16_swap(__float_as_uint(x), __float_as_uint(x), false, false); return __uint_as_float(r[0]) + __uint_as_float(r[1]); }
DEV float x32_add(float x) { auto r = __builtin_amdgcn_permlane32_swap(__float_as_uint(x), __float_as_uint(x), false, false); return __uint_as_float(r[0]) + __uint_as_float(r[1]); }
DEV float x16_max(float x) { auto r = __builtin_amdgcn_permlane16_swap(__float_as_uint(x), __float_as_uint(x), false, false); return fmaxf(__uint_as_float(r[0]), __uint_as_float(r[1])); }
DEV float x32_max(float x) { auto r = __builtin_amdgcn_permlane32_swap(__float_as_uint(x), __float_as_uint(x), false, false); return fmaxf(__uint_as_float(r[0]), __uint_as_float(r[1])); }
DEV unsigned swap23(unsigned r) { return (r & ~12u) | ((r & 4) << 1) | ((r & 8) >> 1); }
DEV int mod7(int v) { return v >= 7 ? v - 7 : v; }
constexpr int slot_of(int s) { return (s + 2) % 3 * kSlot; }
constexpr int w2row(int i) { return (16 * (i >> 1) + 4 * (i & 1)) * 896; }

struct Stg {
  unsigned vo[6], dst[6];
  const char *x, *wmat, *xb, *wb, *first;
  int ks, kstep;
  DEV void bases() { xb = x + ks * 128; wb = wmat + ks * kstep; ks = ks == 6 ? 0 : ks + 1; }
};
struct P2 {
  unsigned v2, v2o;
  unsigned aw[4];
  unsigned sA0, sA1, sA2, sB;
  const char *w2w, *wow;
  int rot;
  bf16x8 Wf[5][4];
  unsigned fofs;
  DEV void peek(const char* ws) const { glds_dev<2>(fofs, ws, kScr + 1408 + (sA0 >> 12) * 1536); }
  DEV const char* src(int i) const { if (i >= 7) return wow + (i - 7) * (28 * 896); const int s = i + rot; return w2w + (s >= 7 ? s - 7 : s) * (32 * 896); }
  DEV void ld1(int step, int i, unsigned slot) const { glds(step >= 7 && i == 3 ? v2o : v2, src(step) + w2row(i), slot + i * 1024); }
  DEV void ld(int step, unsigned slot) const {
#pragma unroll
    for (int i = 0; i < 4; ++i) ld1(step, i, slot);
  }
  DEV void rd1(int b, int u, unsigned slot) { Wf[b][u] = lds_tr(slot + aw[u], slot + aw[u] + 1024); }
  DEV void rd(int b, unsigned slot) {
#pragma unroll
    for (int u = 0; u < 4; ++u) rd1(b, u, slot);
  }
};

template <bool FFN, int NT>
DEV void phase1(Stg& sg, const char* b1, char* ws, u64 tag64, int lane, int w, int rt, int cg, P2& p2 STAMP_ARG) {
  constexpr int WOFF = FFN ? 16384 : 18432, WROW = FFN ? 448 : 384;
  const int mh = w & 1, m = lane & 15, g = lane >> 4, q = m >> 2, p = lane & 3;
  const int t0 = FFN ? (NT == 4 ? (w >> 1) * 4 : 8 + ((w >> 1) - 2) * 3) : (w >> 1) * 3;
  auto tile = [&](int i) { return t0 + (NT == 4 ? i ^ (2 * mh) : (i + 2 * mh) % 3); };
  const int r0 = rt * 128;
  unsigned (&vo)[6] = sg.vo, (&dst)[6] = sg.dst;
  const char *&xb = sg.xb, *&wb = sg.wb;
  auto bases = [&]() { sg.bases(); };
  auto ld_m0 = [&](int i) {
    if (i < 5) glds_m0(vo[i], (i & 1) || i == 4 ? wb : xb);
    else glds_part_m0<FFN ? 32 : 16>(vo[5], FFN ? wb : xb);
  };
  unsigned ax[2], ae[2], aw[NT];
#pragma unroll
  for (int kk = 0; kk < 2; ++kk) { ax[kk] = slot_of(0) + (64 * mh + m) * 128 + (((4 * kk + g) ^ (m >> 1)) << 4); ae[kk] = ax[kk] + (8 - 4 * mh) * 2048; }
#pragma unroll
  for (int i = 0; i < NT; ++i)
    aw[i] = slot_of(0) + WOFF + (4 * (g & 1) + q + 16 * (g >> 1)) * WROW + (p >> 1) * 16 + (p & 1) * 8 + (tile(i) ^ (FFN ? g & 1 : 2 * (g & 1) + (q >> 1))) * 32;
  const unsigned tag = (unsigned)tag64;
  f32x4 acc[4][NT], ace[2];
  bf16x8 X[2][4], W[2][NT], XE[2];
  constexpr int NRD = NT + 2 + (FFN ? 0 : 1);
  auto rd1 = [&](int kk, int i) {
    if (i == 0) { X[kk][0] = lds_b128(ax[kk]); X[kk][1] = lds_b128(ax[kk] + 2048); }
    else if (i == 2) { X[kk][2] = lds_b128(ax[kk] + 4096); X[kk][3] = lds_b128(ax[kk] + 6144); }
    else if (i < NT + 2) { const int n = i == 1 ? 0 : i - 2; W[kk][n] = lds_tr(aw[n] + kk * 32 * WROW, aw[n] + (kk * 32 + 8) * WROW); }
    else if (i < NRD) XE[kk] = lds_b128(ae[kk]);
  };
  const u32x4 one = {0x3f803f80u, 0x3f803f80u, 0x3f803f80u, 0x3f803f80u};
  auto half = [&](int kk, bool first, int nex, bool reads, int adv, int l0, int nl, unsigned slot, auto hook) {
    const f32x4 z = {0.f, 0.f, 0.f, 0.f};
    u32x4 e = one;
#pragma unroll
    for (int i = 0; i < 4 * NT + 4; ++i) {
      if (i >= 4 * NT + nex) break;
      const int sp = nl == 4 ? 3 : 4, l = l0 + i / sp;
      const bool here = i % sp == 1 && i / sp < nl;
      if (here) { set_m0(slot, dst[l]); PIN(); }
      if (!FFN && i == 4 * NT + 2) e = m == 0 ? one : __builtin_bit_cast(u32x4, XE[1 - kk]);
      if (i < 4 * NT) acc[i & 3][i >> 2] = mfma(W[kk][i >> 2], X[kk][i & 3], first ? z : acc[i & 3][i >> 2]);
      else ace[i & 1] = mfma(W[i < 4 * NT + 2 ? kk : 1 - kk][i & 1], __builtin_bit_cast(bf16x8, e), first ? z : ace[i & 1]);
      PIN();
      if (here) ld_m0(l);
      if (reads && i % 2 == 0 && i / 2 < NRD) {
        const int r = i / 2;
        if (adv) {
          if (r == 0) { ax[0] += adv; OPAQUE(ax[0]); }
          else if (r == 2) { ax[1] += adv; OPAQUE(ax[1]); }
          else if (r < NT + 2) { const int n = r == 1 ? 0 : r - 2; aw[n] += adv; OPAQUE(aw[n]); }
          else { ae[0] += adv; ae[1] += adv; OPAQUE(ae[0]); OPAQUE(ae[1]); }
        }
        rd1(1 - kk, r);
      }
      if (!FFN && nex && i == 4 * NT - 3) e = m == 0 ? one : __builtin_bit_cast(u32x4, XE[kk]);
      hook(i);
      PIN();
    }
  };
  const unsigned scr = kScr + w * 1536;
  auto ldstats = [&](int i) {
    if (i == 0) glds_dev<64>(lane * 16, ws + kWsStats + (r0 + 64 * mh) * 16, scr);
    else if (FFN) glds_dev<2 * NT>(lane * 16, b1 + (cg * 14 + t0) * 32, scr + 1024);
    else glds_dev<16>(rt == 0 ? 0 : lane * 16, ws + kWsStats + (rt == 0 ? 0 : r0 - 16) * 16, scr + 1024);
  };
  u32x4 st[5];
  u32x2 bias[NT];
  f32x4 cs[NT];
  auto rdstats = [&](int i) { st[i] = *(LAS u32x4*)(size_t)(scr + (i < 4 ? (i >> 1) * 512 + (i & 1) * 16 + m * 32 : 1024 + m * 16)); };

  WAIT_VM(S1LATE ? 1 : 7);
  CS(2, 4);
  BARRIER();
  STAMP(1);
  CSTAMP(0);
  CS(2, 5);
#pragma unroll
  for (int i = 0; i < NRD; ++i) rd1(0, i);
  bases();
#pragma unroll
  for (int i = 0; i < 3; ++i) { set_m0(slot_of(2), dst[i]); asm volatile("s_nop 0"); ld_m0(i); }
  PIN();
#pragma unroll
  for (int s = 0; s < 7; ++s) {
    if (s == 6) { CSTAMP(2); WAIT_VM(3); }
    half(0, s == 0, s < 6 ? 2 : 4, true, 0, 3, s < 5 ? 3 : 1, slot_of(s + 2), [&](int i) {
      if (s == 5 && (i == 5 || i == 9)) ldstats(i == 9);
      if (s == 6 && (i & 1) && i / 2 < (FFN ? 4 : 5)) rdstats(i / 2);
      if (s == 6 && FFN && i >= 9 && i < 9 + NT) bias[i - 9] = *(LAS u32x2*)(size_t)(scr + 1024 + (tile(i - 9) - t0) * 32 + g * 8);
    });
    if (FFN && s == TOUCH_S) touch(lane * 128, sg.first, scr + 1280, 0xffff);
    if (s == 6) {
      auto stale = [&]() {
        unsigned bad = 0;
#pragma unroll
        for (int i = 0; i < (FFN ? 4 : 5); ++i) { bad |= (st[i][1] ^ tag) | (st[i][3] ^ tag); OPAQUE(bad); }
        return __ballot(bad != 0) != 0;
      };
      if (__builtin_expect(stale(), 0)) {
#pragma nounroll
        for (int spin = 0; spin < kSpinCap; ++spin) {
          __builtin_amdgcn_s_sleep(2);
          ldstats(0);
          if (!FFN) ldstats(1);
          WAIT_VM(0);
#pragma unroll
          for (int i = 0; i < (FFN ? 4 : 5); ++i) rdstats(i);
          if (!stale()) break;
        }
      }
      if (m == 0) {
#pragma unroll
        for (int n = 0; n < 2; ++n) *(LAS f32x4*)(size_t)(kCex + (tile(n) * 16 + 4 * g) * 4) = ace[n];
      }
    }
    WAIT_LGKM0();
    if (s < 6) WAIT_VM(6 + (FFN && s == TOUCH_S));
    BARRIER();
    if (s < 6) CS(6, s < 6 ? s : 0);
    if (s >= 1) CS(7, s >= 1 ? s - 1 : 0);
    if (s < 4) bases();
    else {
      xb = wb = p2.src(s - 4);
      if (s == 4) {
#pragma unroll
        for (int i = 0; i < 4; ++i) { vo[i] = p2.v2 + w2row(i); dst[i] = w * 4096 + i * 1024; }
      }
    }
    PIN();
    if (s < 6) half(1, false, 2, true, slot_of(s + 1) - slot_of(s), 0, 3, slot_of(s), [&](int) {});
    else {
      CSTAMP(3);
      half(1, false, 0, false, 0, 0, 4, p2.sA2 - w * 4096, [&](int i) {
        if (i < NT) cs[i] = lds_f4(kCex + (tile(i) * 16 + 4 * g) * 4);
        else if (i < NT + 4) p2.rd1(0, i - NT, p2.sA0);
      });
    }
  }
  CSTAMP(4);
  CS(3, 0);
  CS(4, 0);
  STAMP(2);
  WAIT_VM(4);
  p2.rd(1, p2.sA1);
  WAIT_LGKM0();
  p2.ld(3, p2.sA0);
  if (FFN && W4N >= 4) p2.ld(4, p2.sA1);
  if (FFN && W4N >= 8) p2.ld(5, p2.sB);
  CS(3, 1);
  const char* a2 = ws + kWsA2;
  const unsigned flag = kWsFlags + ((rt * 16 + cg + (FFN ? 0 : 8)) * 2 + (FFN ? w & 1 : w >> 2)) * 128 + (FFN ? w >> 1 : w & 3) * 8;
  if (FFN) {
    float rs[4], mu[4];
#pragma unroll
    for (int mt = 0; mt < 4; ++mt) { rs[mt] = __uint_as_float(st[mt][2]); mu[mt] = -rs[mt] * __uint_as_float(st[mt][0]); }
    const unsigned sto = (g & 1) * 1024 + ((g >> 1) * 16 + m) * 16;
    const char* hb = a2 + ((2 * rt + mh) * 72 + 7 * cg) * 4096;
#pragma unroll
    for (int n = 0; n < NT; ++n) {
      const f32x2 b0 = {blo(bias[n][0]), bhi(bias[n][0])}, b1v = {blo(bias[n][1]), bhi(bias[n][1])};
      const int T = tile(n);
#pragma unroll
      for (int pp = 0; pp < 2; ++pp) {
        unsigned y[2][2];
#pragma unroll
        for (int e = 0; e < 2; ++e) {
          const int mt = 2 * pp + e;
          f32x2 u0 = fma2(rs[mt], lo2(acc[mt][n]), fma2(mu[mt], lo2(cs[n]), b0)), u1 = fma2(rs[mt], hi2(acc[mt][n]), fma2(mu[mt], hi2(cs[n]), b1v));
          u0 = f32x2{fmaxf(u0[0], 0.f), fmaxf(u0[1], 0.f)}; u1 = f32x2{fmaxf(u1[0], 0.f), fmaxf(u1[1], 0.f)};
          y[e][0] = pack2(u0 * u0); y[e][1] = pack2(u1 * u1);
        }
        const auto s0 = __builtin_amdgcn_permlane16_swap(y[0][0], y[1][0], false, false);
        const auto s1 = __builtin_amdgcn_permlane16_swap(y[0][1], y[1][1], false, false);
        store_wt(sto, hb + (T >> 1) * 4096 + (T & 1) * 512 + pp * 2048, u32x4{s0[0], s1[0], s0[1], s1[1]});
      }
    }
    STAMP(3);
    CS(3, 2);
    WAIT_VM(0);
    store_flag(flag, ws, tag64);
    STAMP(4);
    CS(3, 3);
    BARRIER();
    CS(3, 4);
    p2.rd(2, p2.sA2);
    p2.rd(3, p2.sA0);
    if (W4N < 4) p2.ld(4, p2.sA1);
    if (W4N < 8) p2.ld(5, p2.sB);
    WAIT_LGKM0();
    p2.ld(6, p2.sA2);
    p2.ld(7, p2.sA0);
    if (W4N < 4) WAIT_VM(12);
    p2.rd(4, p2.sA1);
    p2.peek(ws);
    WAIT_LGKM0();
    p2.ld(8, p2.sA1);
    CS(3, 5);
  } else {
    unsigned co[3];
    bool zero[3];
#pragma unroll
    for (int i = 0; i < 3; ++i) {
      const int T = tile(i), n = 16 * (T & 3) + 4 * g;
      co[i] = kAtt + (T >> 2) * 128 + (n + (n < 28 ? 0 : n < 56 ? 4 : n < 60 ? -28 : 0)) * 2;
      zero[i] = n >= 56;
    }
    const unsigned rowm = (8 + 32 * mh + m) * 400, rowe = ((m & 1) * 72 + (m >> 1)) * 400;
    auto put = [&](const f32x4& a, int n, const u32x4& s, unsigned addr) {
      const float nm = -__uint_as_float(s[0]), rs = __uint_as_float(s[2]);
      u32x2 v = {pack2(fma2(nm, lo2(cs[n]), lo2(a)) * rs), pack2(fma2(nm, hi2(cs[n]), hi2(a)) * rs)};
      if (zero[n]) v = u32x2{0u, 0u};
      *(LAS u32x2*)(size_t)addr = v;
    };
#pragma unroll
    for (int n = 0; n < 2; ++n) put(ace[n], n, st[4], co[n] + rowe);
#pragma unroll
    for (int n = 0; n < 3; ++n) co[n] += rowm;
#pragma unroll
    for (int mt = 0; mt < 4; ++mt) {
#pragma unroll
      for (int n = 0; n < 3; ++n) put(acc[mt][n], n, st[mt], co[n] + ((mt & 1) * 72 + 16 * (mt >> 1)) * 400);
      p2.ld1(4, mt, p2.sA1);
    }
    const int blk = w >> 1, par = w & 1;
    const unsigned rb = kAtt + (par * 72 + 16 * blk) * 400;
    unsigned ak0 = rb + m * 400 + 16 * g, ak1 = rb + (16 + (m & 7)) * 400 + 128 + 16 * g;
    unsigned av0 = rb + (4 * g + q) * 400 + 256 + 16 * p, av1 = rb + (16 + 4 * (g & 1) + q) * 400 + 256 + 16 * p;
    OPAQUE(ak0); OPAQUE(ak1); OPAQUE(av0); OPAQUE(av1);
    const int jmin = (16 - (r0 + 32 * blk + par) + 1) >> 1;
    const float kLog = 0.18898223650461362f * 1.4426950408889634f;
    const f32x4 z = {0.f, 0.f, 0.f, 0.f};
    WAIT_LGKM0();
    CS(4, 1);
    BARRIER();
    CS(4, 2);
    f32x4 sc[2][2];
    bf16x8 qf[2], kf[2][2], vf[2][2];
#pragma unroll
    for (int hd = 0; hd < 2; ++hd) { qf[hd] = lds_b128(ak0 + 8 * 400 + hd * 64); kf[hd][0] = lds_b128(ak0 + 128 + hd * 64); kf[hd][1] = lds_b128(ak1 + hd * 64); }
#pragma unroll
    for (int hd = 0; hd < 2; ++hd) { vf[hd][0] = lds_tr(av0 + hd * 64, av1 + hd * 64); vf[hd][1] = lds_tr(av0 + hd * 64 + 8, av1 + hd * 64 + 8); }
    WAIT_VM(8);
    p2.rd(2, p2.sA2);
    PIN();
#pragma unroll
    for (int hd = 0; hd < 2; ++hd) { sc[hd][0] = mfma(kf[hd][0], qf[hd], z); sc[hd][1] = mfma(kf[hd][1], qf[hd], z); }
    WAIT_LGKM0();
    BARRIER();
    CS(4, 3);
#pragma unroll
    for (int hd = 0; hd < 2; ++hd) {
      const int step = 5 + hd;
      const unsigned slot = hd ? p2.sA2 : p2.sB;
      float e[8], mx = -3.0e38f;
      p2.ld1(step, 0, slot);
#pragma unroll
      for (int i = 0; i < 8; ++i) {
        const int j = 16 * (i >> 2) + 4 * g + (i & 3);
        const float v = j < jmin ? -1.0e9f * 1.4426950408889634f : sc[hd][i >> 2][i & 3] * kLog;
        e[i] = (unsigned)(m + 8 - j) < 8u ? v : -__builtin_inff();
        mx = fmaxf(mx, e[i]);
      }
      p2.ld1(step, 1, slot);
      mx = x32_max(x16_max(mx));
      float sum = 0.f;
#pragma unroll
      for (int i = 0; i < 8; ++i) { e[i] = __builtin_amdgcn_exp2f(e[i] - mx); sum += e[i]; }
      p2.ld1(step, 2, slot);
      const float inv = __builtin_amdgcn_rcpf(x32_add(x16_add(sum)));
      const u32x4 pf = {pack2(e[0] * inv, e[1] * inv), pack2(e[2] * inv, e[3] * inv), pack2(e[4] * inv, e[5] * inv), pack2(e[6] * inv, e[7] * inv)};
      const f32x4 o0 = mfma(vf[hd][0], __builtin_bit_cast(bf16x8, pf), z), o1 = mfma(vf[hd][1], __builtin_bit_cast(bf16x8, pf), z);
      p2.ld1(step, 3, slot);
      store_wt(lane * 16, a2 + (((2 * rt + (blk >> 1)) * 72 + 56 + 2 * cg + hd) * 4 + 2 * (blk & 1) + par) * 1024,
               u32x4{pack2(lo2(o0)), pack2(hi2(o0)), pack2(lo2(o1)), pack2(hi2(o1))});
    }
    STAMP(3);
    CS(4, 4);
    WAIT_VM(10);
    p2.rd(3, p2.sA0);
    p2.rd(4, p2.sA1);
    WAIT_LGKM0();
    p2.ld(7, p2.sA0);
    WAIT_VM(4);
    store_flag(flag, ws, tag64);
    p2.peek(ws);
    p2.ld(8, p2.sA1);
    STAMP(4);
    CS(4, 5);
  }
}

__global__ __launch_bounds__(512, 2) void fused_kernel(const char*, const char*, const char*, const char*, const char*, const char*, const char*, char*, char*) {
  STAMP_INIT();
  STAMP(0);
  const char *x, *wqkv, *wo, *w1, *b1, *w2, *b2;
  char *out, *ws;
  u32x16 ka;
  u64 kb;
  asm volatile("s_load_dwordx16 %0, %2, 0x0\n\ts_load_dwordx2 %1, %2, 0x40" : "=&s"(ka), "=&s"(kb) : "s"(__builtin_amdgcn_kernarg_segment_ptr()));
  __shared__ __attribute__((aligned(16))) char smem[kLds];
  asm volatile("" ::"v"((LAS char*)smem));
  const int lane = threadIdx.x & 63, w = __builtin_amdgcn_readfirstlane(threadIdx.x >> 6);
  const int bid = blockIdx.x, xcd = bid & 7, j = bid >> 3;
  {
    const char* pc;
    unsigned nl;
    asm volatile("s_getpc_b64 %0\n.Lfk_pc:\n\ts_mov_b32 %1, (.Lfk_end - .Lfk_pc) / 128" : "=s"(pc), "=s"(nl));
    const unsigned li = (j * 8 + w) * 2 + lane;
    touch(li * 128, pc, kScr + w * 1536 + 1280, __ballot(li < nl) & 3);
  }
  const int rt = (xcd & 1) * 8 + (j & 7), cgi = j >> 3, cg = 2 * (xcd >> 1) + (cgi & 1), r0 = rt * 128;
  const bool ffn = cgi < 2;
  Stg sg;
  u64 tag64;
  {
    const unsigned grow = ffn ? 3584 : 2688, qpr4 = ffn ? 28 : 24, magic = ffn ? 9363 : 10923, xsh = ffn ? 1 : 0, xmsk = ffn ? 2 : 6;
    const unsigned sec = ffn ? 128 : 896, cmax = ffn ? 7 : 6, cofs = ffn ? cg * 448 : cg * 112, woff0 = ffn ? 16384 : 18432;
    auto woff = [&](unsigned L) -> unsigned {
      const unsigned rho = __umul24(L >> 2, magic) >> 16, c = (L - __umul24(rho, qpr4)) ^ ((rho >> xsh) & xmsk);
      return __umul24(swap23(rho), grow) + __umul24(c >> 3, sec) + (min(c & 7, cmax) << 4) + cofs;
    };
#pragma unroll
    for (int u = 0; u < 2; ++u) {
      const unsigned r = 8 * u + (lane >> 3);
      sg.vo[2 * u] = __umul24(r0 + 32 * (w >> 1) + 2 * r + (w & 1), 896) + (((lane & 7) ^ (r >> 1)) << 4);
      sg.dst[2 * u] = w * 2048 + u * 1024;
    }
    sg.vo[1] = woff(192 * w + lane);
    sg.dst[1] = woff0 + w * 3072; sg.dst[3] = sg.dst[1] + 1024; sg.dst[4] = sg.dst[1] + 2048;
    sg.dst[5] = ffn ? 16384 + 24576 + w * 512 : 16384 + w * 256;
    sg.ks = ROT == 0 ? 0 : ROT == 1 ? (rt + 2 * cgi + (xcd >> 1)) % 7 : ROT == 2 ? ((rt + cgi) & 1) * 3 : ROT == 3 ? ((rt & 1) * 3 + (xcd >> 1)) % 7 : ROT == 4 ? (rt % 3) * 2 : ROT == 5 ? (rt + (xcd >> 1)) % 7 : ROT == 6 ? mod7((rt & 3) * 2 + (xcd >> 1)) : mod7(2 * (xcd >> 1) + (xcd & 1) * 3);
    sg.kstep = 64 * grow;
    const int strow = (xcd & 1) * 1024 + (xcd >> 1) * 256 + j * 8 + w;
    unsigned srow_off = strow * 896 + (lane < 56 ? lane : 55) * 16;
    asm volatile("" : "+v"(sg.vo[0]), "+v"(sg.vo[1]), "+v"(sg.vo[2]), "+v"(srow_off), "+s"(sg.ks));
    asm volatile("s_waitcnt lgkmcnt(0)" : "+s"(ka), "+s"(kb));
    auto ptr = [&](int i) { return (char*)(((u64)ka[2 * i + 1] << 32) | ka[2 * i]); };
    x = ptr(0); wqkv = ptr(1); wo = ptr(2); w1 = ptr(3); b1 = ptr(4); w2 = ptr(5); b2 = ptr(6); out = ptr(7); ws = (char*)kb;
    CS(2, 0);
    sg.x = x;
    sg.wmat = ffn ? w1 : wqkv;
    const u64 part = ffn ? 0xffffffffull : 0xffffull;
    auto ld = [&](int s, int i) {
      set_m0(slot_of(s), sg.dst[i]);
      if (i < 5) { asm volatile("s_nop 0"); glds_m0(sg.vo[i], (i & 1) || i == 4 ? sg.wb : sg.xb); }
      else asm volatile("s_mov_b64 exec, %2\n\tglobal_load_lds_dwordx4 %0, %1\n\ts_mov_b64 exec, -1" ::"v"(sg.vo[5]), "s"(ffn ? sg.wb : sg.xb), "s"(part) : "memory");
    };
    u32x2 ep;
    u32x4 srow;
    asm volatile("s_mov_b64 exec, 1\n\tglobal_load_dwordx2 %0, %1, %2 sc1\n\ts_mov_b64 exec, -1" : "=v"(ep) : "v"(0), "s"(ws) : "memory");
    asm volatile("global_load_dwordx4 %0, %1, %2" : "=v"(srow) : "v"(srow_off), "s"(x) : "memory");
    sg.bases();
    ld(0, 0); ld(0, 1); ld(0, 2);
    sg.vo[3] = woff(192 * w + 64 + lane);
    ld(0, 3);
    sg.vo[4] = woff(192 * w + 128 + lane);
    ld(0, 4);
    const int hr = r0 - 16 + 2 * w + ((lane >> 3) & 1);
    unsigned v5f = woff(1536 + 32 * w + (lane & 31)), v5q = __umul24(hr < 0 ? 0 : hr, 896) + (((lane & 7) ^ w) << 4);
    OPAQUE(v5f); OPAQUE(v5q);
    sg.vo[5] = ffn ? v5f : v5q;
    ld(0, 5);
    CS(2, 1);
    asm volatile("s_waitcnt vmcnt(6)" : "+v"(srow), "+v"(ep)::"memory");
    tag64 = (((u64)__builtin_amdgcn_readfirstlane(ep[1]) << 32) | (unsigned)__builtin_amdgcn_readfirstlane(ep[0])) + 1;
    const float piv = blo(__builtin_amdgcn_readfirstlane(srow[0]));
    f32x2 sm2 = {0.f, 0.f}, sq2 = {0.f, 0.f};
#pragma unroll
    for (int i = 0; i < 4; ++i) { const f32x2 v = f32x2{blo(srow[i]), bhi(srow[i])} - piv; sm2 += v; sq2 = __builtin_elementwise_fma(v, v, sq2); }
    float sm = lane < 56 ? sm2[0] + sm2[1] : 0.f, sq = lane < 56 ? sq2[0] + sq2[1] : 0.f;
    sm += dpp<0xB1>(sm); sq += dpp<0xB1>(sq);
    sm += dpp<0x4E>(sm); sq += dpp<0x4E>(sq);
    sm += dpp<0x141>(sm); sq += dpp<0x141>(sq);
    sm += dpp<0x140>(sm); sq += dpp<0x140>(sq);
    sm = x16_add(sm); sq = x16_add(sq);
    sm = x32_add(sm) * (1.f / 448.f); sq = x32_add(sq) * (1.f / 448.f);
    store_wt_lane0(kWsStats + strow * 16, ws,
                   u32x4{__float_as_uint(piv + sm), (unsigned)tag64, __float_as_uint(__builtin_amdgcn_rsqf(fmaxf(fmaf(-sm, sm, sq), 0.f) + 1e-5f)), (unsigned)tag64});
    CS(2, 3);
    sg.bases();
#pragma unroll
    for (int i = 0; i < 6; ++i) ld(1, i);
    const int l0 = ((rt * 8 + cg) * 8 + w) * 16;
    sg.first = (l0 < 12544 ? w2 : wo) + (l0 < 12544 ? l0 : l0 < 15680 ? l0 - 12544 : 0) * 128;
    asm volatile("" : "+s"(sg.first));
    CS(2, 2);
  }
  const int rg = 4 * xcd + (j & 3), ct = mod7(mod7(7 + (j < 28 ? j >> 2 : 6) - 2 * (j & 3)));
  const bool idle2 = j >= 28;
  const int m = lane & 15, g = lane >> 4, q = m >> 2, p = lane & 3, r7 = lane >> 3;
  P2 p2;
  p2.v2 = (8 * (r7 >> 2) + (r7 & 3)) * 896 + (((lane & 7) ^ (r7 & 6)) << 4);
#pragma unroll
  for (int u = 0; u < 4; ++u) p2.aw[u] = (4 * (g & 1) + q + 16 * (g >> 1)) * 128 + ((u ^ (2 * (g & 1) + (q >> 1))) * 32) + (p >> 1) * 16 + (p & 1) * 8;
  p2.v2o = lane < 32 ? p2.v2 : p2.v2 - 4 * 896;
  p2.sA0 = w * 4096; p2.sA1 = kSlot + w * 4096; p2.sA2 = (w < 3 ? 32768 : 65536) + w * 4096; p2.sB = 98304 + w * 4096;
  p2.w2w = w2 + w * (224 * 896) + ct * 128;
  p2.wow = wo + w * (56 * 896) + ct * 128;
  p2.rot = 2 * (rg & 3);
  p2.fofs = kWsFlags + (((rg >> 1) * 16 + w) * 2 + (rg & 1)) * 128 + (lane & 1) * 16;

  if (!ffn) phase1<false, 3>(sg, b1, ws, tag64, lane, w, rt, cg, p2 STAMP_PASS);
  else if (w < 4) phase1<true, 4>(sg, b1, ws, tag64, lane, w, rt, cg, p2 STAMP_PASS);
  else phase1<true, 3>(sg, b1, ws, tag64, lane, w, rt, cg, p2 STAMP_PASS);

  if (idle2) {
    if (bid == 255 && w == 0) {
      const unsigned t0 = (unsigned)tag64, t1 = (unsigned)(tag64 >> 32);
#pragma nounroll
      for (int k = 0, spin = 0; k < 16 && spin < kSpinCap; ++spin) {
        u32x4 f;
        asm volatile("global_load_dwordx4 %0, %1, %2 sc1\n\ts_waitcnt vmcnt(0)" : "=&v"(f) : "v"((lane >> 1) * 128 + (lane & 1) * 16), "s"(ws + kWsFlags + k * 4096) : "memory");
        if (__all(f[0] == t0 && f[1] == t1 && f[2] == t0 && f[3] == t1)) ++k;
        else __builtin_amdgcn_s_sleep(2);
      }
      store_flag(0, ws, tag64);
    }
    STAMP_OUT();
    return;
  }

  const int os = threadIdx.x >> 3, orow = rg * 64 + 32 * (os >> 5) + 2 * (os & 15) + ((os >> 4) & 1);
  const unsigned oofs = __umul24(orow, 896) + ct * 128 + (threadIdx.x & 7) * 16;
  u32x4 xr, br;
  CS(5, 0);
  const unsigned fofs = kWsFlags + (((rg >> 1) * 16 + w) * 2 + (rg & 1)) * 128 + (lane & 3) * 8;
  auto ready = [&](u32x2 f) { return (__ballot(f[0] == (unsigned)tag64 && f[1] == (unsigned)(tag64 >> 32)) & 15) == 15; };
  auto poll = [&](unsigned ofs) {
#pragma nounroll
    for (int spin = 0; spin < kSpinCap; ++spin) {
      u32x2 f;
      asm volatile("s_mov_b64 exec, 15\n\tglobal_load_dwordx2 %0, %1, %2 sc1\n\ts_mov_b64 exec, -1\n\ts_waitcnt vmcnt(0)" : "=&v"(f) : "v"(ofs), "s"(ws) : "memory");
      if (ready(f)) break;
      __builtin_amdgcn_s_sleep(1);
    }
  };
  WAIT_VM(4);
  if (!ready(*(LAS u32x2*)(size_t)(kScr + 1408 + w * 1536 + (lane & 3) * 8))) poll(fofs);
  STAMP(5);
  const char* a2 = ws + kWsA2 + rg * (72 * 4096);
  u32x4 H[3][4];
  auto ldh = [&](int b, int i) {
    const int s = i + p2.rot;
    const char* src = a2 + (i < 7 ? 7 * w + (s >= 7 ? s - 7 : s) : 56 + 2 * w + (i - 7)) * 4096;
#pragma unroll
    for (int mt = 0; mt < 4; ++mt) asm volatile("global_load_dwordx4 %0, %1, %2 offset:%3 sc1" : "=v"(H[b][mt]) : "v"(lane * 16), "s"(src), "i"(mt * 1024) : "memory");
  };
  CS(5, 1);
  ldh(0, 0); ldh(1, 1); ldh(2, 2);
  f32x4 acc[4][4];
  u32x2 fq;
#pragma unroll
  for (int i = 0; i < 9; ++i) {
    const int b = i % 3;
    constexpr int kWait[9] = {8, 8, 8, 9, 9, 10, 10, 6, 0};
    asm volatile("s_waitcnt vmcnt(%4)" : "+v"(H[b][0]), "+v"(H[b][1]), "+v"(H[b][2]), "+v"(H[b][3]) : "i"(kWait[i]) : "memory");
    if (i >= 1 && i < 5) p2.rd(i - 1, i == 1 ? p2.sB : i == 2 ? p2.sA2 : i == 3 ? p2.sA0 : p2.sA1);
    const f32x4 z = {0.f, 0.f, 0.f, 0.f};
#pragma unroll
    for (int mt = 0; mt < 4; ++mt)
#pragma unroll
      for (int u = 0; u < 4; ++u) acc[mt][u] = mfma(p2.Wf[i % 5][u], __builtin_bit_cast(bf16x8, H[b][mt]), i == 0 ? z : acc[mt][u]);
    PIN();
    if (i == 4) {
      asm volatile("s_waitcnt vmcnt(4)" : "+v"(fq)::"memory");
      if (!ready(fq)) poll(fofs + 2048);
    }
    if (i + 3 < 9) ldh(b, i + 3);
    if (i == 2) asm volatile("s_mov_b64 exec, 15\n\tglobal_load_dwordx2 %0, %1, %2 offset:2048 sc1\n\ts_mov_b64 exec, -1" : "=v"(fq) : "v"(fofs), "s"(ws) : "memory");
    if (i == 4) {
      asm volatile("global_load_dwordx4 %0, %1, %2" : "=v"(xr) : "v"(oofs), "s"(x) : "memory");
      asm volatile("global_load_dwordx4 %0, %1, %2" : "=v"(br) : "v"(ct * 128 + (threadIdx.x & 7) * 16), "s"(b2) : "memory");
    }
  }
  OPAQUE(xr); OPAQUE(br);
  CS(5, 2);
  STAMP(6);
#pragma unroll
  for (int mt = 0; mt < 4; ++mt) {
    const unsigned a = (mt == 0 ? p2.sA0 : mt == 1 ? p2.sA1 : mt == 2 ? p2.sB : p2.sA2) + m * 256;
#pragma unroll
    for (int u = 0; u < 4; ++u) *(LAS f32x4*)(size_t)(a + (((4 * u + g) ^ (m & 7)) << 4)) = acc[mt][u];
  }
  f32x4 lo = {blo(xr[0]) + blo(br[0]), bhi(xr[0]) + bhi(br[0]), blo(xr[1]) + blo(br[1]), bhi(xr[1]) + bhi(br[1])};
  f32x4 hi = {blo(xr[2]) + blo(br[2]), bhi(xr[2]) + bhi(br[2]), blo(xr[3]) + blo(br[3]), bhi(xr[3]) + bhi(br[3])};
  unsigned ra[2][2];
  ra[0][0] = (w < 2 ? 0 : w < 4 ? kSlot : w < 6 ? 98304 : 32768) + (os & 15) * 256 + (((2 * (threadIdx.x & 7)) ^ (os & 7)) << 4);
  ra[0][1] = ra[0][0] ^ 16;
  ra[1][0] = ra[0][0] + (w < 6 ? 0 : 32768);
  ra[1][1] = ra[1][0] ^ 16;
  OPAQUE(lo); OPAQUE(hi); OPAQUE(ra[0][0]); OPAQUE(ra[0][1]); OPAQUE(ra[1][0]); OPAQUE(ra[1][1]);
  WAIT_LGKM0();
  CS(5, 3);
  BARRIER();
  CS(5, 4);
#pragma unroll
  for (int k = 0; k < 8; ++k) { lo += lds_f4(ra[k >= 3][0] + k * 4096); hi += lds_f4(ra[k >= 3][1] + k * 4096); }
  *(__attribute__((address_space(1))) u32x4*)(out + oofs) = u32x4{pack2(lo2(lo)), pack2(hi2(lo)), pack2(lo2(hi)), pack2(hi2(hi))};
  CS(5, 5);
  STAMP(7);
  STAMP_OUT();
  asm volatile(".Lfk_end:");
}

extern "C" void kernel_launch(void* const* d_in, const int* in_sizes, int n_in, void* d_out, int out_size, void* d_ws, size_t ws_size, hipStream_t stream) {
  fused_kernel<<<256, 512, 0, stream>>>((const char*)d_in[0], (const char*)d_in[1], (const char*)d_in[2], (const char*)d_in[4], (const char*)d_in[5],
                                        (const char*)d_in[6], (const char*)d_in[7], (char*)d_out, (char*)d_ws);
}
```

```cpp
#include <hip/hip_runtime.h>
#include <hip/hip_bf16.h>

#define FW 1
#define PRIO 4
typedef __bf16 bf16x2 __attribute__((ext_vector_type(2)));
typedef __bf16 bf16x4 __attribute__((ext_vector_type(4)));
typedef __bf16 bf16x8 __attribute__((ext_vector_type(8)));
typedef float f32x2 __attribute__((ext_vector_type(2)));
typedef float f32x4 __attribute__((ext_vector_type(4)));
typedef unsigned u32x2 __attribute__((ext_vector_type(2)));
typedef unsigned u32x4 __attribute__((ext_vector_type(4)));
typedef unsigned u32x16 __attribute__((ext_vector_type(16)));
typedef unsigned long long u64;
#define LAS __attribute__((address_space(3)))
#define DEV __device__ __forceinline__

constexpr int kLds = 163840;
constexpr int kSlot = 45056;
constexpr int kCex = kLds - 1024;
constexpr int kAtt = kCex - 144 * 400;
constexpr int kScr = 3 * kSlot, kScrW = 1536;
constexpr int kWsFlags = 4096, kWsStats = 131072, kWsA2 = 262144, kWsDiag = 16 << 20;
constexpr int kSpinCap = 1 << 16;
constexpr unsigned kOut = 0x80000000u;

#define STAMP(i)
#define STAMP_ARG
#define STAMP_PASS
#define STAMP_INIT()
#define STAMP_OUT()
#define CSTAMP(i)
#define CS(set, i)

#define PIN() __builtin_amdgcn_sched_barrier(0)
#define OPAQUE(x) asm volatile("" : "+v"(x))
#define OPAQUE_S(x) asm volatile("" : "+s"(x))
#define NOP(n) asm volatile("s_nop %0" ::"i"(n))
#define WAIT_VM(n) asm volatile("s_waitcnt vmcnt(%0)" ::"i"(n) : "memory")
#define WAIT_LGKM0() do { asm volatile("" ::: "memory"); __builtin_amdgcn_s_waitcnt(0xC07F); asm volatile("" ::: "memory"); } while (0)
#define BARRIER() do { asm volatile("" ::: "memory"); __builtin_amdgcn_s_barrier(); asm volatile("" ::: "memory"); } while (0)

DEV u32x4 make_srd(const char* p, unsigned bytes) { return u32x4{(unsigned)(u64)p, (unsigned)((u64)p >> 32) & 0xffffu, bytes, 0x00020000u}; }
DEV void set_m0(unsigned a, unsigned b) {
  if (__builtin_constant_p(a)) asm volatile("s_add_u32 m0, %1, %0" ::"i"(a), "s"(b) : "scc");
  else asm volatile("s_add_u32 m0, %0, %1" ::"s"(a), "s"(b) : "scc");
}
DEV void dma(unsigned voff, u32x4 srd, unsigned soff, int ofs) {
  asm volatile("buffer_load_dwordx4 %0, %1, %2 offen offset:%3 lds" ::"v"(voff), "s"(srd), "s"(soff), "i"(ofs) : "memory");
}
DEV void dma_part(unsigned voff, u32x4 srd, unsigned soff, u64 lanes) {
  asm volatile("s_mov_b64 exec, %3\n\tbuffer_load_dwordx4 %0, %1, %2 offen lds\n\ts_mov_b64 exec, -1" ::"v"(voff), "s"(srd), "s"(soff), "s"(lanes) : "memory");
}
DEV void dma_dev(unsigned voff, const char* sbase, unsigned dst, u64 lanes) {
  asm volatile("s_mov_b32 m0, %2\n\ts_mov_b64 exec, %3\n\tglobal_load_lds_dwordx4 %0, %1 sc1\n\ts_mov_b64 exec, -1" ::"v"(voff), "s"(sbase), "s"(dst), "s"(lanes) : "memory");
}
DEV void touch(unsigned voff, const char* sbase, unsigned dst, u64 lanes) {
  asm volatile("s_mov_b32 m0, %2\n\ts_mov_b64 exec, %3\n\tglobal_load_lds_dword %0, %1\n\ts_mov_b64 exec, -1" ::"v"(voff), "s"(sbase), "s"(dst), "s"(lanes) : "memory");
}
DEV void store_wt(unsigned voff, u32x4 srd, unsigned soff, int ofs, u32x4 v) {
  asm volatile("buffer_store_dwordx4 %0, %1, %2, %3 offen offset:%4 sc1\n\ts_nop 1" ::"v"(v), "v"(voff), "s"(srd), "s"(soff), "i"(ofs) : "memory");
}
DEV void store_wt_lane0(unsigned voff, const char* sbase, u32x4 v) {
  asm volatile("s_mov_b64 exec, 1\n\tglobal_store_dwordx4 %0, %1, %2 sc1\n\ts_mov_b64 exec, -1\n\ts_nop 0" ::"v"(voff), "v"(v), "s"(sbase) : "memory");
}
DEV void store_flag(unsigned voff, const char* sbase, u64 v) {
  asm volatile("s_mov_b64 exec, 1\n\tglobal_store_dwordx2 %0, %1, %2 sc1\n\ts_mov_b64 exec, -1" ::"v"(voff), "v"(v), "s"(sbase) : "memory");
}

DEV bf16x8 lds_b128(unsigned a) { return *(LAS bf16x8*)(size_t)a; }
DEV f32x4 lds_f4(unsigned a) { return *(LAS f32x4*)(size_t)a; }
DEV bf16x8 lds_tr(unsigned a0, unsigned a1) {
  bf16x4 a = __builtin_amdgcn_ds_read_tr16_b64_v4bf16((LAS bf16x4*)(size_t)a0);
  bf16x4 b = __builtin_amdgcn_ds_read_tr16_b64_v4bf16((LAS bf16x4*)(size_t)a1);
  return __builtin_shufflevector(a, b, 0, 1, 2, 3, 4, 5, 6, 7);
}
DEV f32x4 mfma(bf16x8 a, bf16x8 b, f32x4 c) { return __builtin_amdgcn_mfma_f32_16x16x32_bf16(a, b, c, 0, 0, 0); }
template <class A, class B> DEV void mma(f32x4& c, A a, B b, bool first) {
  if (first) asm volatile("v_mfma_f32_16x16x32_bf16 %0, %1, %2, 0" : "=&v"(c) : "v"(a), "v"(b));
  else asm volatile("v_mfma_f32_16x16x32_bf16 %0, %1, %2, %0" : "+v"(c) : "v"(a), "v"(b));
}
DEV unsigned pack2(f32x2 v) { return __builtin_bit_cast(unsigned, __builtin_convertvector(v, bf16x2)); }
DEV float blo(unsigned u) { return __uint_as_float(u << 16); }
DEV float bhi(unsigned u) { return __uint_as_float(u & 0xffff0000u); }
DEV f32x2 lo2(f32x4 v) { return __builtin_shufflevector(v, v, 0, 1); }
DEV f32x2 hi2(f32x4 v) { return __builtin_shufflevector(v, v, 2, 3); }
DEV f32x2 fma2(float a, f32x2 b, f32x2 c) { return __builtin_elementwise_fma(f32x2{a, a}, b, c); }
template <int C> DEV float dpp(float x) { return __builtin_bit_cast(float, __builtin_amdgcn_update_dpp(0, __builtin_bit_cast(int, x), C, 0xF, 0xF, true)); }
DEV float x16_add(float x) { auto r = __builtin_amdgcn_permlane16_swap(__float_as_uint(x), __float_as_uint(x), false, false); return __uint_as_float(r[0]) + __uint_as_float(r[1]); }
DEV float x32_add(float x) { auto r = __builtin_amdgcn_permlane32_swap(__float_as_uint(x), __float_as_uint(x), false, false); return __uint_as_float(r[0]) + __uint_as_float(r[1]); }
DEV float x16_max(float x) { auto r = __builtin_amdgcn_permlane16_swap(__float_as_uint(x), __float_as_uint(x), false, false); return fmaxf(__uint_as_float(r[0]), __uint_as_float(r[1])); }
DEV float x32_max(float x) { auto r = __builtin_amdgcn_permlane32_swap(__float_as_uint(x), __float_as_uint(x), false, false); return fmaxf(__uint_as_float(r[0]), __uint_as_float(r[1])); }
DEV float max3(float a, float b, float c) { float r; asm("v_max3_f32 %0, %1, %2, %3" : "=v"(r) : "v"(a), "v"(b), "v"(c)); return r; }
template <class F> DEV void rows2(float& a, float& b, F f) {
  auto r = __builtin_amdgcn_permlane16_swap(__float_as_uint(a), __float_as_uint(b), false, false);
  const unsigned x = __float_as_uint(f(__uint_as_float(r[0]), __uint_as_float(r[1])));
  r = __builtin_amdgcn_permlane32_swap(x, x, false, false);
  const unsigned y = __float_as_uint(f(__uint_as_float(r[0]), __uint_as_float(r[1])));
  r = __builtin_amdgcn_permlane16_swap(y, y, false, false);
  a = __uint_as_float(r[0]); b = __uint_as_float(r[1]);
}
DEV unsigned swap23(unsigned r) { return (r & ~12u) | ((r & 4) << 1) | ((r & 8) >> 1); }
DEV int mod7(int v) { return v >= 7 ? v - 7 : v; }
constexpr int slot_of(int s) { return (s + 2) % 3 * kSlot; }

struct Stg {
  unsigned vo[6], dx, dp, dw;
  u32x4 xs, ws;
  unsigned xo, wo;
  int ks, kstep;
  const char* first;
  DEV void next() { xo = ks * 128; wo = ks * kstep; ks = ks == 6 ? 0 : ks + 1; }
};
struct P2 {
  unsigned va[4], vpad;
  unsigned aw[3][4];
  unsigned sA0, sA2;
  u32x4 s2, so, sa;
  int rot;
  bf16x8 Wf[5][4];
  unsigned flag;
  unsigned fofs;
  DEV void peek(const char* ws) const { dma_dev(fofs, ws, kScr + 1408 + (sA0 >> 12) * kScrW, 3); }
  DEV int kstep(int s) const { return mod7(s + rot); }
  DEV void m0(int s) const { if (s == 2 || s == 6) set_m0(0, sA2); else set_m0(s == 5 ? 98304 : s == 1 || s == 4 || s == 8 ? kSlot : 0, sA0); }
  DEV void go(int s, int i) const {
    if (s < 7) dma(va[i], s2, kstep(s) * (32 * 896), i * 1024);
    else dma(i == 3 ? vpad : va[i], so, (s - 7) * (28 * 896), i * 1024);
  }
  DEV void ld(int s) const {
    m0(s); NOP(0);
#pragma unroll
    for (int i = 0; i < 4; ++i) go(s, i);
  }
  DEV void rd1(int s, int u) {
    const unsigned a = s == 2 || s == 6 ? aw[1][u] : s == 5 ? aw[2][u] : aw[0][u] + (s == 1 || s == 4 || s == 8 ? kSlot : 0);
    Wf[s % 5][u] = lds_tr(a, a + 1024);
  }
  DEV void rd(int s) {
#pragma unroll
    for (int u = 0; u < 4; ++u) rd1(s, u);
  }
};

template <bool FFN, int NT>
DEV void phase1(Stg& sg, const char* b1, char* ws, u64 tag64, int lane, int w, int rt, int cg, P2& p2 STAMP_ARG) {
  constexpr int WOFF = FFN ? 16384 : 18432, WROW = FFN ? 448 : 384;
  const int mh = w & 1, m = lane & 15, g = lane >> 4, q = m >> 2, p = lane & 3;
  const int t0 = FFN ? (NT == 4 ? (w >> 1) * 4 : 8 + ((w >> 1) - 2) * 3) : (w >> 1) * 3;
  auto tile = [&](int i) { return t0 + (NT == 4 ? i ^ (2 * mh) : i == 0 ? 2 * mh : i == 1 ? 1 - mh : 2 - mh); };
  const int r0 = rt * 128;
  auto ld_m0 = [&](int l, unsigned slot) { if (l == 0 || l == 2 || l == 3) set_m0(slot, l == 0 ? sg.dx : l == 2 ? sg.dp : sg.dw); };
  auto ld_go = [&](int l) {
    if (l < 2) dma(sg.vo[l], sg.xs, sg.xo, l * 1024);
    else if (l == 2) dma_part(sg.vo[2], FFN ? sg.ws : sg.xs, FFN ? sg.wo : sg.xo, FFN ? 0xffffffffull : 0xffffull);
    else dma(sg.vo[l], sg.ws, sg.wo, (l - 3) * 1024);
  };
  unsigned ax[2], ae[2], aw[NT];
#pragma unroll
  for (int kk = 0; kk < 2; ++kk) { ax[kk] = slot_of(0) + (64 * mh + m) * 128 + (((4 * kk + g) ^ (m >> 1)) << 4); ae[kk] = ax[kk] + (8 - 4 * mh) * 2048; }
#pragma unroll
  for (int i = 0; i < NT; ++i)
    aw[i] = slot_of(0) + WOFF + (4 * (g & 1) + q + 16 * (g >> 1)) * WROW + (p >> 1) * 16 + (p & 1) * 8 + (tile(i) ^ (FFN ? g & 1 : 2 * (g & 1) + (q >> 1))) * 32;
  const unsigned scr = kScr + w * kScrW;
  unsigned ast = scr + m * 32, acx[NT], abi[NT], sto = (g & 1) * 1024 + ((g >> 1) * 16 + m) * 16, sso[NT];
  unsigned co[3], ce[2], ak0, ak1, av0, av1, oso;
  f32x2 km[4], kb[4];
  u32x4 one = {0x3f803f80u, 0x3f803f80u, 0x3f803f80u, 0x3f803f80u};
  OPAQUE(one); OPAQUE(ast);
#pragma unroll
  for (int n = 0; n < NT; ++n) { acx[n] = kCex + (tile(n) * 16 + 4 * g) * 4; OPAQUE(acx[n]); }
  if (FFN) {
    OPAQUE(sto);
#pragma unroll
    for (int n = 0; n < NT; ++n) {
      abi[n] = scr + 1024 + (tile(n) - t0) * 32 + g * 8; OPAQUE(abi[n]);
      sso[n] = ((2 * rt + mh) * 72 + 7 * cg + (tile(n) >> 1)) * 4096 + (tile(n) & 1) * 512; OPAQUE_S(sso[n]);
    }
  }
  auto prep = [&](int i) {
    if (FFN) return;
    const int blk = w >> 1, par = w & 1;
    const unsigned rb = kAtt + (par * 72 + 16 * blk) * 400;
    if (i < 3) {
      const int T = tile(i), n = 16 * (T & 3) + 4 * g;
      const unsigned c = kAtt + (T >> 2) * 128 + (n + (n < 28 ? 0 : n < 56 ? 4 : n < 60 ? -28 : 0)) * 2;
      co[i] = c + (8 + 32 * mh + m) * 400; OPAQUE(co[i]);
      if (i < 2) { ce[i] = c + ((m & 1) * 72 + (m >> 1)) * 400; OPAQUE(ce[i]); }
    } else if (i == 3) { ak0 = rb + m * 400 + 16 * g; ak1 = rb + (16 + (m & 7)) * 400 + 128 + 16 * g; OPAQUE(ak0); OPAQUE(ak1); }
    else if (i == 4) { av0 = rb + (4 * g + q) * 400 + 256 + 16 * p; av1 = rb + (16 + 4 * (g & 1) + q) * 400 + 256 + 16 * p; OPAQUE(av0); OPAQUE(av1); }
    else if (i < 13) {
      const int k = i - 5, jo = 16 * (k >> 2) + (k & 3), jmin = (16 - (r0 + 32 * blk + par) + 1) >> 1;
      const bool in = (unsigned)(m + 8 - 4 * g - jo) < 8u, pre = 4 * g < jmin - jo;
      float a = in && !pre ? 0.18898223650461362f * 1.4426950408889634f : 0.f, b = !in ? -__builtin_inff() : pre ? -1.0e9f * 1.4426950408889634f : 0.f;
      OPAQUE(a); OPAQUE(b);
      km[k >> 1][k & 1] = a; kb[k >> 1][k & 1] = b;
    }
  };
  oso = (((2 * rt + (w >> 2)) * 72 + 56 + 2 * cg) * 4 + (w & 3)) * 1024;
  const unsigned tag = (unsigned)tag64;
  f32x4 acc[4][NT], ace[2];
  bf16x8 X[2][4], W[2][NT], XE[2];
  constexpr int NRD = NT + 2 + (FFN ? 0 : 1);
  auto rd1 = [&](int kk, int i) {
    if (i == 0) { X[kk][0] = lds_b128(ax[kk]); X[kk][1] = lds_b128(ax[kk] + 2048); }
    else if (i == 2) { X[kk][2] = lds_b128(ax[kk] + 4096); X[kk][3] = lds_b128(ax[kk] + 6144); }
    else if (i < NT + 2) { const int n = i == 1 ? 0 : i - 2; W[kk][n] = lds_tr(aw[n] + kk * 32 * WROW, aw[n] + (kk * 32 + 8) * WROW); }
    else if (i < NRD) XE[kk] = lds_b128(ae[kk]);
  };
  auto half = [&](int kk, bool first, int nex, bool reads, int adv, auto load_at, unsigned slot, auto hook) {
    u32x4 e = one;
#pragma unroll
    for (int i = 0; i < 4 * NT + 4; ++i) {
      if (i >= 4 * NT + nex) break;
      const int l = load_at(i);
      if (l >= 100) { if ((l & 3) == 0) p2.m0((l - 100) >> 2); } else if (l >= 0) ld_m0(l, slot);
      if (!FFN && i == 4 * NT + 2) { e = m == 0 ? one : __builtin_bit_cast(u32x4, XE[1 - kk]); OPAQUE(e); NOP(1); }
      if (i < 4 * NT) mma(acc[i & 3][i >> 2], W[kk][i >> 2], X[kk][i & 3], first);
      else mma(ace[i & 1], W[i < 4 * NT + 2 ? kk : 1 - kk][i & 1], e, first);
      if (l >= 100) p2.go((l - 100) >> 2, l & 3); else if (l >= 0) ld_go(l);
      if (reads && i % 2 == 0 && i / 2 < NRD) {
        const int r = i / 2;
        if (adv) {
          if (r == 0) { ax[0] += adv; OPAQUE(ax[0]); }
          else if (r == 2) { ax[1] += adv; OPAQUE(ax[1]); }
          else if (r < NT + 2) { const int n = r == 1 ? 0 : r - 2; aw[n] += adv; OPAQUE(aw[n]); }
          else { ae[0] += adv; ae[1] += adv; OPAQUE(ae[0]); OPAQUE(ae[1]); }
        }
        rd1(1 - kk, r);
      }
      if (!FFN && nex && i == 4 * NT - 3) { e = m == 0 ? one : __builtin_bit_cast(u32x4, XE[kk]); OPAQUE(e); }
      hook(i);
      PIN();
    }
  };
  auto ldstats = [&](int i) {
    if (i == 0) dma_dev(lane * 16, ws + kWsStats + (r0 + 64 * mh) * 16, scr, ~0ull);
    else if (FFN) dma_dev(lane * 16, b1 + (cg * 14 + t0) * 32, scr + 1024, (1ull << (2 * NT)) - 1);
    else dma_dev(rt == 0 ? 0 : lane * 16, ws + kWsStats + (rt == 0 ? 0 : r0 - 16) * 16, scr + 1024, 0xffffull);
  };
  u32x4 st[5];
  u32x2 bias[NT];
  f32x4 cs[NT];
  unsigned bad = 0;
  auto rdstats = [&](int i) { st[i] = *(LAS u32x4*)(size_t)(i < 4 ? ast + (i >> 1) * 512 + (i & 1) * 16 : scr + 1024 + m * 16); };
  auto check = [&](int i) { bad |= (st[i][1] ^ tag) | (st[i][3] ^ tag); OPAQUE(bad); };
  constexpr int NST = FFN ? 4 : 5;

  WAIT_VM(7);
  CS(2, 4);
  BARRIER();
  STAMP(1);
  CSTAMP(0);
  CS(2, 5);
#pragma unroll
  for (int i = 0; i < NRD; ++i) rd1(0, i);
  sg.next();
#pragma unroll
  for (int l = 0; l < 3; ++l) { ld_m0(l, slot_of(2)); NOP(0); ld_go(l); }
  PIN();
#pragma unroll
  for (int s = 0; s < 6; ++s) {
    auto three = [](int l0) { return [=](int i) { return i % 4 == 1 && i < 12 ? l0 + i / 4 : -1; }; };
    auto four = [](int l0) { return [=](int i) { return i % 3 == 1 && i < 12 ? l0 + i / 3 : -1; }; };
    auto none = [](int) { return -1; };
    if (s < 5) half(0, s == 0, 2, true, 0, three(3), slot_of(s + 2), [&](int i) { if (s == 3 && (i == 11 || i == 13)) ldstats(i == 13); });
    else half(0, false, 2, true, 0, none, 0, [&](int i) {
      if ((i & 1) && i / 2 < NST) rdstats(i / 2);
      if (i >= 9 && i < 9 + NST) check(i - 9);
    });
    if (FFN && s == 1) touch(lane * 128, sg.first, scr + 1280, 0xffff);
    if (s == 5 && __builtin_expect(__ballot(bad != 0) != 0, 0)) {
#pragma nounroll
      for (int spin = 0; spin < kSpinCap; ++spin) {
        __builtin_amdgcn_s_sleep(2);
        ldstats(0);
        if (!FFN) ldstats(1);
        WAIT_VM(0);
        bad = 0;
#pragma unroll
        for (int i = 0; i < NST; ++i) { rdstats(i); check(i); }
        if (__ballot(bad != 0) == 0) break;
      }
    }
    WAIT_LGKM0();
    if (s < 5) WAIT_VM(6 + (FFN && s == 1) + (s == 3 ? 2 : 0));
    else WAIT_VM(4);
    BARRIER();
    if (s < 6) CS(6, s < 6 ? s : 0);
    if (s >= 1) CS(7, s >= 1 ? s - 1 : 0);
    if (s < 4) sg.next();
    PIN();
    if (s < 4) half(1, false, 2, true, slot_of(s + 1) - slot_of(s), three(0), slot_of(s), [&](int i) { if (i % 4 == 3 || i == 13) prep(4 * s + (i == 13 ? 3 : i / 4)); });
    else half(1, false, 2, true, slot_of(s + 1) - slot_of(s), four(100 + 4 * (s - 4)), 0, [&](int) {});
  }
  CSTAMP(2);
  auto blk4 = [&](int kk, int n, auto hook) {
#pragma unroll
    for (int mt = 0; mt < 4; ++mt) { mma(acc[mt][n], W[kk][n], X[kk][mt], false); hook(mt); PIN(); }
  };
  {
    u32x4 e0 = one, e1 = one;
    blk4(0, 0, [&](int i) { rd1(1, i == 0 ? 1 : i == 1 ? 3 : i == 2 ? (FFN ? 0 : NRD - 1) : (FFN ? 2 : 0)); });
    if (!FFN) { e0 = m == 0 ? one : __builtin_bit_cast(u32x4, XE[0]); OPAQUE(e0); }
    blk4(0, 1, [&](int i) {
      if (FFN) { if (i + 4 < NRD) rd1(1, i + 4); bias[i < NT ? i : 0] = *(LAS u32x2*)(size_t)abi[i < NT ? i : 0]; }
      else if (i < 2) rd1(1, i == 0 ? 2 : 4);
    });
    mma(ace[0], W[0][0], e0, false); mma(ace[1], W[0][1], e0, false);
    if (!FFN) { e1 = m == 0 ? one : __builtin_bit_cast(u32x4, XE[1]); OPAQUE(e1); NOP(1); }
    mma(ace[0], W[1][0], e1, false); mma(ace[1], W[1][1], e1, false);
    PIN();
    blk4(0, 2, [&](int) {});
    NOP(3);
    asm volatile("s_mov_b64 exec, %4\n\tds_write_b128 %0, %1\n\tds_write_b128 %2, %3\n\ts_mov_b64 exec, -1\n\ts_waitcnt lgkmcnt(0)"
                 ::"v"(acx[0]), "v"(ace[0]), "v"(acx[1]), "v"(ace[1]), "s"(0x0001000100010001ull) : "memory");
  }
  WAIT_LGKM0();
  BARRIER();
  CS(7, 5);
  CSTAMP(3);
  if (PRIO & 8) asm volatile("s_cmp_lt_u32 %0, 4\n\ts_cbranch_scc1 1f\n\ts_setprio 1\n1:" ::"s"(w) : "scc");
  if (!FFN || FW >= 1) p2.m0(2);
  blk4(1, 0, [&](int i) { if (!FFN || FW >= 1) p2.go(2, i); if (i < NT) cs[i] = lds_f4(acx[i]); });
  if (FFN && FW >= 2) p2.m0(5);
  blk4(1, 1, [&](int i) { if (FFN && FW >= 2) p2.go(5, i); });
  float rs[5], mu[5];
#pragma unroll
  for (int i = 0; i < NST; ++i) { rs[i] = __uint_as_float(st[i][2]); mu[i] = FFN ? -rs[i] * __uint_as_float(st[i][0]) : -__uint_as_float(st[i][0]); }
  auto epi = [&](int n) {
    if (FFN) {
      const f32x2 b0 = {blo(bias[n][0]), bhi(bias[n][0])}, b1v = {blo(bias[n][1]), bhi(bias[n][1])};
#pragma unroll
      for (int pp = 0; pp < 2; ++pp) {
        unsigned y[2][2];
#pragma unroll
        for (int e = 0; e < 2; ++e) {
          const int mt = 2 * pp + e;
          f32x2 u0 = fma2(rs[mt], lo2(acc[mt][n]), fma2(mu[mt], lo2(cs[n]), b0)), u1 = fma2(rs[mt], hi2(acc[mt][n]), fma2(mu[mt], hi2(cs[n]), b1v));
          u0 = f32x2{fmaxf(u0[0], 0.f), fmaxf(u0[1], 0.f)}; u1 = f32x2{fmaxf(u1[0], 0.f), fmaxf(u1[1], 0.f)};
          y[e][0] = pack2(u0 * u0); y[e][1] = pack2(u1 * u1);
        }
        const auto s0 = __builtin_amdgcn_permlane16_swap(y[0][0], y[1][0], false, false);
        const auto s1 = __builtin_amdgcn_permlane16_swap(y[0][1], y[1][1], false, false);
        store_wt(sto, p2.sa, sso[n], pp * 2048, u32x4{s0[0], s1[0], s0[1], s1[1]});
      }
    } else {
      auto put = [&](const f32x4& a, int i, unsigned addr) {
        *(LAS u32x2*)(size_t)addr = u32x2{pack2(fma2(mu[i], lo2(cs[n]), lo2(a)) * rs[i]), pack2(fma2(mu[i], hi2(cs[n]), hi2(a)) * rs[i])};
      };
      if (n < 2) put(ace[n], 4, ce[n]);
#pragma unroll
      for (int mt = 0; mt < 4; ++mt) put(acc[mt][n], mt, co[n] + ((mt & 1) * 72 + 16 * (mt >> 1)) * 400);
    }
    PIN();
  };
  PIN();
  NOP(3);
  PIN();
  epi(0);
  blk4(1, 2, [&](int) {});
  epi(1);
  if (NT == 4) { blk4(0, 3, [&](int) {}); blk4(1, 3, [&](int) {}); }
  epi(2);
  if (NT == 4) epi(3);
  CSTAMP(4);
  CS(3, 0);
  CS(4, 0);
  STAMP(2);
  if (FFN) {
    STAMP(3);
    CS(3, 2);
    WAIT_VM(0);
    store_flag(p2.flag, ws, tag64);
    STAMP(4);
    CS(3, 3);
    p2.rd(0);
    p2.rd(1);
    if (FW >= 1) p2.rd(2);
    WAIT_LGKM0();
    BARRIER();
    CS(3, 4);
    if (FW == 0) {
      p2.ld(2); p2.ld(3); p2.ld(4); p2.ld(5);
      WAIT_VM(8); p2.rd(2); p2.rd(3); WAIT_LGKM0();
      p2.ld(6); p2.ld(7);
      WAIT_VM(12); p2.rd(4);
    } else {
      p2.ld(3); p2.ld(4);
      if (FW == 1) p2.ld(5);
      p2.ld(6);
      WAIT_VM(FW == 1 ? 12 : 8); p2.rd(3); WAIT_LGKM0();
      p2.ld(7);
      WAIT_VM(FW == 1 ? 12 : 8); p2.rd(4);
    }
    p2.peek(ws);
    WAIT_LGKM0();
    p2.ld(8);
    CS(3, 5);
  } else {
    WAIT_VM(4);
    p2.rd(0);
    p2.rd(1);
    WAIT_LGKM0();
    CS(4, 1);
    BARRIER();
    CS(4, 2);
    p2.ld(3);
    const f32x4 z = {0.f, 0.f, 0.f, 0.f};
    f32x4 sc[2][2];
    bf16x8 qf[2], kf[2][2], vf[2][2];
#pragma unroll
    for (int hd = 0; hd < 2; ++hd) { qf[hd] = lds_b128(ak0 + 8 * 400 + hd * 64); kf[hd][0] = lds_b128(ak0 + 128 + hd * 64); kf[hd][1] = lds_b128(ak1 + hd * 64); }
#pragma unroll
    for (int hd = 0; hd < 2; ++hd) { vf[hd][0] = lds_tr(av0 + hd * 64, av1 + hd * 64); vf[hd][1] = lds_tr(av0 + hd * 64 + 8, av1 + hd * 64 + 8); }
    PIN();
#pragma unroll
    for (int hd = 0; hd < 2; ++hd) { sc[hd][0] = mfma(kf[hd][0], qf[hd], z); sc[hd][1] = mfma(kf[hd][1], qf[hd], z); }
    PIN();
    p2.ld(4);
    WAIT_VM(8);
    p2.rd(2);
    WAIT_LGKM0();
    BARRIER();
    CS(4, 3);
    p2.ld(5);
    PIN();
    f32x2 e[2][4];
    float mx[2], sum[2];
#pragma unroll
    for (int hd = 0; hd < 2; ++hd) {
#pragma unroll
      for (int t = 0; t < 2; ++t) {
        e[hd][2 * t] = __builtin_elementwise_fma(lo2(sc[hd][t]), km[2 * t], kb[2 * t]);
        e[hd][2 * t + 1] = __builtin_elementwise_fma(hi2(sc[hd][t]), km[2 * t + 1], kb[2 * t + 1]);
      }
      mx[hd] = max3(max3(e[hd][0][0], e[hd][0][1], e[hd][1][0]), max3(e[hd][1][1], e[hd][2][0], e[hd][2][1]), max3(e[hd][3][0], e[hd][3][1], e[hd][3][1]));
    }
    rows2(mx[0], mx[1], [](float a, float b) { return max3(a, b, b); });
    PIN();
    p2.ld(6);
    PIN();
#pragma unroll
    for (int hd = 0; hd < 2; ++hd) {
#pragma unroll
      for (int i = 0; i < 4; ++i) { const f32x2 d = e[hd][i] - mx[hd]; e[hd][i] = f32x2{__builtin_amdgcn_exp2f(d[0]), __builtin_amdgcn_exp2f(d[1])}; }
      const f32x2 t = (e[hd][0] + e[hd][1]) + (e[hd][2] + e[hd][3]);
      sum[hd] = t[0] + t[1];
    }
    rows2(sum[0], sum[1], [](float a, float b) { return a + b; });
    f32x4 o[2][2];
#pragma unroll
    for (int hd = 0; hd < 2; ++hd) {
      const float inv = __builtin_amdgcn_rcpf(sum[hd]);
      const u32x4 pf = {pack2(e[hd][0] * inv), pack2(e[hd][1] * inv), pack2(e[hd][2] * inv), pack2(e[hd][3] * inv)};
      o[hd][0] = mfma(vf[hd][0], __builtin_bit_cast(bf16x8, pf), z);
      o[hd][1] = mfma(vf[hd][1], __builtin_bit_cast(bf16x8, pf), z);
    }
    WAIT_VM(8);
    p2.rd(3);
    p2.rd(4);
#pragma unroll
    for (int hd = 0; hd < 2; ++hd)
      store_wt(lane * 16, p2.sa, oso + hd * 4096, 0, u32x4{pack2(lo2(o[hd][0])), pack2(hi2(o[hd][0])), pack2(lo2(o[hd][1])), pack2(hi2(o[hd][1]))});
    STAMP(3);
    CS(4, 4);
    WAIT_LGKM0();
    p2.ld(7);
    WAIT_VM(4);
    store_flag(p2.flag, ws, tag64);
    p2.peek(ws);
    p2.ld(8);
    STAMP(4);
    CS(4, 5);
  }
}

__global__ __launch_bounds__(512, 2) void fused_kernel(const char*, const char*, const char*, const char*, const char*, const char*, const char*, char*, char*) {
  STAMP_INIT();
  STAMP(0);
  u32x16 ka;
  u64 kb;
  asm volatile("s_load_dwordx16 %0, %2, 0x0\n\ts_load_dwordx2 %1, %2, 0x40" : "=&s"(ka), "=&s"(kb) : "s"(__builtin_amdgcn_kernarg_segment_ptr()));
  __shared__ __attribute__((aligned(16))) char smem[kLds];
  asm volatile("" ::"v"((LAS char*)smem));
  const int lane = threadIdx.x & 63, w = __builtin_amdgcn_readfirstlane(threadIdx.x >> 6);
  const int bid = blockIdx.x, xcd = bid & 7, j = bid >> 3;
  if (PRIO & 1) asm volatile("s_cmp_lt_u32 %0, 4\n\ts_cbranch_scc1 1f\n\ts_setprio 1\n1:" ::"s"(w) : "scc");
  {
    const char* pc;
    unsigned nl;
    asm volatile("s_getpc_b64 %0\n.Lfk_pc:\n\ts_mov_b32 %1, (.Lfk_end - .Lfk_pc) / 128" : "=s"(pc), "=s"(nl));
    const unsigned li = (j * 8 + w) * 2 + lane;
    touch(li * 128, pc, kScr + w * kScrW + 1280, __ballot(li < nl) & 3);
  }
  const int rt = (xcd & 1) * 8 + (j & 7), cgi = j >> 3, cg = 2 * (xcd >> 1) + (cgi & 1);
  const bool ffn = cgi < 2;
  const char *x, *wo, *b1, *w2, *b2;
  char *out, *ws;
  Stg sg;
  u64 tag64;
  {
    const int r0 = rt * 128;
#pragma unroll
    for (int u = 0; u < 2; ++u) {
      const unsigned r = 8 * u + (lane >> 3);
      sg.vo[u] = __umul24(r0 + 32 * (w >> 1) + 2 * r + (w & 1), 896) + (((lane & 7) ^ (r >> 1)) << 4) - u * 1024;
    }
    sg.dx = w * 2048;
    sg.ks = mod7((rt & 3) * 2 + (xcd >> 1));
    const unsigned grow = ffn ? 3584 : 2688, qpr4 = ffn ? 28 : 24, magic = ffn ? 9363 : 10923, xsh = ffn ? 1 : 0, xmsk = ffn ? 2 : 6;
    const unsigned sec = ffn ? 128 : 896, cmax = ffn ? 7 : 6, cofs = ffn ? cg * 448 : cg * 112;
    auto woff = [&](unsigned L) -> unsigned {
      const unsigned rho = __umul24(L >> 2, magic) >> 16, c = (L - __umul24(rho, qpr4)) ^ ((rho >> xsh) & xmsk);
      return (c & 7) > cmax ? kOut : __umul24(swap23(rho), grow) + __umul24(c >> 3, sec) + ((c & 7) << 4) + cofs;
    };
    sg.vo[3] = woff(192 * w + lane);
    sg.dw = (ffn ? 16384 : 18432) + w * 3072;
    sg.dp = ffn ? 16384 + 24576 + w * 512 : 16384 + w * 256;
    sg.kstep = 64 * grow;
    const int strow = (xcd & 1) * 1024 + (xcd >> 1) * 256 + j * 8 + w;
    unsigned srow_off = strow * 896 + (lane < 56 ? lane : 55) * 16;
    asm volatile("" : "+v"(sg.vo[0]), "+v"(sg.vo[1]), "+v"(sg.vo[3]), "+v"(srow_off), "+s"(sg.ks), "+s"(sg.dx), "+s"(sg.dw));
    asm volatile("s_waitcnt lgkmcnt(0)" : "+s"(ka), "+s"(kb));
    auto ptr = [&](int i) { return (char*)(((u64)ka[2 * i + 1] << 32) | ka[2 * i]); };
    x = ptr(0); wo = ptr(2); b1 = ptr(4); w2 = ptr(5); b2 = ptr(6); out = ptr(7); ws = (char*)kb;
    CS(2, 0);
    sg.xs = make_srd(x, 2048 * 896);
    sg.ws = make_srd(ffn ? ptr(3) : ptr(1), 448 * grow);
    const u32x4 ps = ffn ? sg.ws : sg.xs;
    const u64 part = ffn ? 0xffffffffull : 0xffffull;
    auto ld = [&](int s, int l) {
      if (l == 0 || l == 2 || l == 3) set_m0(slot_of(s), l == 0 ? sg.dx : l == 2 ? sg.dp : sg.dw);
      if (l == 0 || l == 3) NOP(0);
      if (l < 2) dma(sg.vo[l], sg.xs, sg.xo, l * 1024);
      else if (l == 2) dma_part(sg.vo[2], ps, ffn ? sg.wo : sg.xo, part);
      else dma(sg.vo[l], sg.ws, sg.wo, (l - 3) * 1024);
    };
    u32x2 ep;
    u32x4 srow;
    asm volatile("s_mov_b64 exec, 1\n\tglobal_load_dwordx2 %0, %1, %2 sc1\n\ts_mov_b64 exec, -1" : "=v"(ep) : "v"(0), "s"(ws) : "memory");
    asm volatile("global_load_dwordx4 %0, %1, %2" : "=v"(srow) : "v"(srow_off), "s"(x) : "memory");
    sg.next();
    ld(0, 0); ld(0, 1); ld(0, 3);
    sg.vo[4] = woff(192 * w + 64 + lane) - 1024;
    ld(0, 4);
    sg.vo[5] = woff(192 * w + 128 + lane) - 2048;
    ld(0, 5);
    const int hr = r0 - 16 + 2 * w + ((lane >> 3) & 1);
    unsigned v2f = woff(1536 + 32 * w + (lane & 31)), v2q = __umul24(hr < 0 ? 0 : hr, 896) + (((lane & 7) ^ w) << 4);
    OPAQUE(v2f); OPAQUE(v2q);
    sg.vo[2] = ffn ? v2f : v2q;
    ld(0, 2);
    CS(2, 1);
    asm volatile("s_waitcnt vmcnt(6)" : "+v"(srow), "+v"(ep)::"memory");
    tag64 = (((u64)__builtin_amdgcn_readfirstlane(ep[1]) << 32) | (unsigned)__builtin_amdgcn_readfirstlane(ep[0])) + 1;
    const float piv = blo(__builtin_amdgcn_readfirstlane(srow[0]));
    f32x2 sm2 = {0.f, 0.f}, sq2 = {0.f, 0.f};
#pragma unroll
    for (int i = 0; i < 4; ++i) { const f32x2 v = f32x2{blo(srow[i]), bhi(srow[i])} - piv; sm2 += v; sq2 = __builtin_elementwise_fma(v, v, sq2); }
    float sm = lane < 56 ? sm2[0] + sm2[1] : 0.f, sq = lane < 56 ? sq2[0] + sq2[1] : 0.f;
    sm += dpp<0xB1>(sm); sq += dpp<0xB1>(sq);
    sm += dpp<0x4E>(sm); sq += dpp<0x4E>(sq);
    sm += dpp<0x141>(sm); sq += dpp<0x141>(sq);
    sm += dpp<0x140>(sm); sq += dpp<0x140>(sq);
    sm = x16_add(sm); sq = x16_add(sq);
    sm = x32_add(sm) * (1.f / 448.f); sq = x32_add(sq) * (1.f / 448.f);
    store_wt_lane0(kWsStats + strow * 16, ws,
                   u32x4{__float_as_uint(piv + sm), (unsigned)tag64, __float_as_uint(__builtin_amdgcn_rsqf(fmaxf(fmaf(-sm, sm, sq), 0.f) + 1e-5f)), (unsigned)tag64});
    CS(2, 3);
    sg.next();
#pragma unroll
    for (int l = 0; l < 6; ++l) ld(1, l);
    const int l0 = ((rt * 8 + cg) * 8 + w) * 16;
    sg.first = (l0 < 12544 ? w2 : wo) + (l0 < 12544 ? l0 : l0 < 15680 ? l0 - 12544 : 0) * 128;
    OPAQUE_S(sg.first);
    CS(2, 2);
  }
  const int rg = 4 * xcd + (j & 3), ct = mod7(mod7(7 + (j < 28 ? j >> 2 : 6) - 2 * (j & 3)));
  const int m = lane & 15, g = lane >> 4, q = m >> 2, p = lane & 3, r7 = lane >> 3;
  P2 p2;
  p2.sA0 = w * 4096; p2.sA2 = (w < 3 ? 32768 : 65536) + w * 4096;
#pragma unroll
  for (int i = 0; i < 4; ++i) {
    p2.va[i] = (8 * (r7 >> 2) + (r7 & 3) + 16 * (i >> 1) + 4 * (i & 1)) * 896 + (((lane & 7) ^ (r7 & 6)) << 4) - i * 1024;
    const unsigned a = (4 * (g & 1) + q + 16 * (g >> 1)) * 128 + ((i ^ (2 * (g & 1) + (q >> 1))) * 32) + (p >> 1) * 16 + (p & 1) * 8;
    p2.aw[0][i] = a + p2.sA0; p2.aw[1][i] = a + p2.sA2; p2.aw[2][i] = a + p2.sA0 + 98304;
    OPAQUE(p2.va[i]); OPAQUE(p2.aw[0][i]); OPAQUE(p2.aw[1][i]); OPAQUE(p2.aw[2][i]);
  }
  p2.vpad = lane < 32 ? p2.va[3] : kOut;
  OPAQUE(p2.vpad);
  p2.s2 = make_srd(w2 + w * (224 * 896) + ct * 128, 224 * 896);
  p2.so = make_srd(wo + w * (56 * 896) + ct * 128, 56 * 896);
  p2.sa = make_srd(ws + kWsA2, 32 * 72 * 4096);
  p2.rot = 2 * (rg & 3);
  p2.flag = kWsFlags + ((rt * 16 + cg + (ffn ? 0 : 8)) * 2 + (ffn ? w & 1 : w >> 2)) * 128 + (ffn ? w >> 1 : w & 3) * 8;
  p2.fofs = kWsFlags + (((rg >> 1) * 16 + w) * 2 + (rg & 1)) * 128 + (lane & 1) * 16;

  if (!ffn) phase1<false, 3>(sg, b1, ws, tag64, lane, w, rt, cg, p2 STAMP_PASS);
  else if (w < 4) phase1<true, 4>(sg, b1, ws, tag64, lane, w, rt, cg, p2 STAMP_PASS);
  else phase1<true, 3>(sg, b1, ws, tag64, lane, w, rt, cg, p2 STAMP_PASS);

  if (j >= 28) {
    if (bid == 255) {
      const unsigned t0 = (unsigned)tag64, t1 = (unsigned)(tag64 >> 32);
#pragma nounroll
      for (int spin = 0; spin < kSpinCap; ++spin) {
        u32x4 f, h;
        asm volatile("global_load_dwordx4 %0, %2, %3 sc1\n\tglobal_load_dwordx4 %1, %2, %4 sc1\n\ts_waitcnt vmcnt(0)" : "=&v"(f), "=&v"(h)
                     : "v"((lane >> 1) * 128 + (lane & 1) * 16), "s"(ws + kWsFlags + w * 8192), "s"(ws + kWsFlags + w * 8192 + 4096) : "memory");
        if (__all(f[0] == t0 && f[1] == t1 && f[2] == t0 && f[3] == t1 && h[0] == t0 && h[1] == t1 && h[2] == t0 && h[3] == t1)) break;
        __builtin_amdgcn_s_sleep(8);
      }
      BARRIER();
      if (w == 0) store_flag(0, ws, tag64);
    }
    STAMP(7);
    STAMP_OUT();
    return;
  }

  const unsigned fofs = kWsFlags + (((rg >> 1) * 16 + w) * 2 + (rg & 1)) * 128 + (lane & 3) * 8;
  auto ready = [&](u32x2 f) { return (__ballot(f[0] == (unsigned)tag64 && f[1] == (unsigned)(tag64 >> 32)) & 15) == 15; };
  auto poll = [&](unsigned ofs) {
#pragma nounroll
    for (int spin = 0; spin < kSpinCap; ++spin) {
      u32x2 f;
      asm volatile("s_mov_b64 exec, 15\n\tglobal_load_dwordx2 %0, %1, %2 sc1\n\ts_mov_b64 exec, -1\n\ts_waitcnt vmcnt(0)" : "=&v"(f) : "v"(ofs), "s"(ws) : "memory");
      if (ready(f)) break;
      __builtin_amdgcn_s_sleep(1);
    }
  };
  CS(5, 0);
  WAIT_VM(4);
  if (!ready(*(LAS u32x2*)(size_t)(kScr + 1408 + w * kScrW + (lane & 3) * 8))) poll(fofs);
  STAMP(5);
  u32x4 H[2][4];
  auto hso = [&](int i) { return (unsigned)(rg * 72 + (i < 7 ? 7 * w + p2.kstep(i) : 56 + 2 * w + (i - 7))) * 4096; };
  auto ldh = [&](int i, int mt, unsigned so) {
    asm volatile("buffer_load_dwordx4 %0, %1, %2, %3 offen offset:%4 sc1" : "=v"(H[i & 1][mt]) : "v"(lane * 16), "s"(p2.sa), "s"(so), "i"(mt * 1024) : "memory");
  };
  CS(5, 1);
  if (PRIO & 4) asm volatile("s_cmp_lt_u32 %0, 4\n\ts_cbranch_scc1 1f\n\ts_setprio 1\n1:" ::"s"(w) : "scc");
  CS(10, 0);
#pragma unroll
  for (int i = 0; i < 2; ++i) {
    const unsigned so = hso(i);
#pragma unroll
    for (int mt = 0; mt < 4; ++mt) ldh(i, mt, so);
  }
  const int os = threadIdx.x >> 3, orow = rg * 64 + 32 * (os >> 5) + 2 * (os & 15) + ((os >> 4) & 1);
  unsigned oofs = __umul24(orow, 896) + ct * 128 + (threadIdx.x & 7) * 16;
  u32x4 xr, br;
  unsigned pa[3][4], ra[2][2];
#pragma unroll
  for (int u = 0; u < 4; ++u) {
    const unsigned a = m * 256 + (((4 * u + g) ^ (m & 7)) << 4);
    pa[0][u] = a + p2.sA0; pa[1][u] = a + p2.sA0 + 98304; pa[2][u] = a + p2.sA2;
    OPAQUE(pa[0][u]); OPAQUE(pa[1][u]); OPAQUE(pa[2][u]);
  }
  ra[0][0] = (w < 2 ? 0 : w < 4 ? kSlot : w < 6 ? 98304 : 32768) + (os & 15) * 256 + (((2 * (threadIdx.x & 7)) ^ (os & 7)) << 4);
  ra[0][1] = ra[0][0] ^ 16;
  ra[1][0] = ra[0][0] + (w < 6 ? 0 : 32768);
  ra[1][1] = ra[1][0] ^ 16;
  OPAQUE(ra[0][0]); OPAQUE(ra[0][1]); OPAQUE(ra[1][0]); OPAQUE(ra[1][1]); OPAQUE(oofs);
  f32x4 acc[4][4], lo, hi;
  u32x2 fq;
  auto park = [&](int mt, int u) { *(LAS f32x4*)(size_t)(mt < 2 ? pa[0][u] + mt * kSlot : pa[mt - 1][u]) = acc[mt][u]; };
#pragma unroll
  for (int i = 0; i < 9; ++i) {
    const unsigned so = i + 2 < 9 ? hso(i + 2) : 0;
    if (i == 5) {
      asm volatile("s_waitcnt vmcnt(6)" : "+v"(fq)::"memory");
      if (!ready(fq)) poll(fofs + 2048);
    }
#pragma unroll
    for (int mt = 0; mt < 4; ++mt) {
      const int younger = (i < 7 ? 7 : i == 7 ? 7 - mt : 3 - mt) + (i == 4 ? 1 : i == 5 ? 2 : 0);
      asm volatile("s_waitcnt vmcnt(%1)" : "+v"(H[i & 1][mt]) : "i"(younger) : "memory");
      if (mt == 0 && i % 2 == 0) CS(10, 1 + i / 2);
#pragma unroll
      for (int u = 0; u < 4; ++u) {
        mma(acc[mt][u], p2.Wf[i % 5][u], H[i & 1][mt], i == 0);
        if (i >= 1 && i < 5 && u == 0) p2.rd1(i + 4, mt);
        if (i == 7 && mt == 0 && u == 0) {
          OPAQUE(xr); OPAQUE(br);
          lo = f32x4{blo(xr[0]) + blo(br[0]), bhi(xr[0]) + bhi(br[0]), blo(xr[1]) + blo(br[1]), bhi(xr[1]) + bhi(br[1])};
          hi = f32x4{blo(xr[2]) + blo(br[2]), bhi(xr[2]) + bhi(br[2]), blo(xr[3]) + blo(br[3]), bhi(xr[3]) + bhi(br[3])};
          OPAQUE(lo); OPAQUE(hi);
        }
        if (i == 8 && mt >= 2) park(mt - 2, u);
        PIN();
      }
      if (i + 2 < 9) ldh(i, mt, so);
    }
    if (i == 3) asm volatile("s_mov_b64 exec, 15\n\tglobal_load_dwordx2 %0, %1, %2 offset:2048 sc1\n\ts_mov_b64 exec, -1" : "=v"(fq) : "v"(fofs), "s"(ws) : "memory");
    if (i == 4) {
      asm volatile("global_load_dwordx4 %0, %1, %2" : "=v"(xr) : "v"(oofs), "s"(x) : "memory");
      asm volatile("global_load_dwordx4 %0, %1, %2" : "=v"(br) : "v"(ct * 128 + (threadIdx.x & 7) * 16), "s"(b2) : "memory");
    }
  }
  CS(5, 2);
  STAMP(6);
#pragma unroll
  for (int u = 0; u < 4; ++u) park(2, u);
  PIN();
  NOP(3);
  PIN();
#pragma unroll
  for (int u = 0; u < 4; ++u) park(3, u);
  WAIT_LGKM0();
  CS(5, 3);
  BARRIER();
  CS(5, 4);
#pragma unroll
  for (int k = 0; k < 8; ++k) { lo += lds_f4(ra[k >= 3][0] + k * 4096); hi += lds_f4(ra[k >= 3][1] + k * 4096); }
  *(__attribute__((address_space(1))) u32x4*)(out + oofs) = u32x4{pack2(lo2(lo)), pack2(hi2(lo)), pack2(lo2(hi)), pack2(hi2(hi))};
  CS(5, 5);
  STAMP(7);
  STAMP_OUT();
  asm volatile(".Lfk_end:");
}

extern "C" void kernel_launch(void* const* d_in, const int* in_sizes, int n_in, void* d_out, int out_size, void* d_ws, size_t ws_size, hipStream_t stream) {
  fused_kernel<<<256, 512, 0, stream>>>((const char*)d_in[0], (const char*)d_in[1], (const char*)d_in[2], (const char*)d_in[4], (const char*)d_in[5],
                                        (const char*)d_in[6], (const char*)d_in[7], (char*)d_out, (char*)d_ws);
}
```

```cpp
#include <hip/hip_runtime.h>
#include <hip/hip_bf16.h>

#define LATE78 0
#define WIN 2
#define PRIO 4
typedef __bf16 bf16x2 __attribute__((ext_vector_type(2)));
typedef __bf16 bf16x4 __attribute__((ext_vector_type(4)));
typedef __bf16 bf16x8 __attribute__((ext_vector_type(8)));
typedef float f32x2 __attribute__((ext_vector_type(2)));
typedef float f32x4 __attribute__((ext_vector_type(4)));
typedef unsigned u32x2 __attribute__((ext_vector_type(2)));
typedef unsigned u32x4 __attribute__((ext_vector_type(4)));
typedef unsigned u32x16 __attribute__((ext_vector_type(16)));
typedef unsigned long long u64;
#define LAS __attribute__((address_space(3)))
#define DEV __device__ __forceinline__

constexpr int kLds = 163840;
constexpr int kSlot = 45056;
constexpr int kCex = kLds - 1024;
constexpr int kAtt = kCex - 144 * 400;
constexpr int kScr = 3 * kSlot, kScrW = 1536;
constexpr int kWsFlags = 4096, kWsStats = 131072, kWsA2 = 262144, kWsDiag = 16 << 20;
constexpr int kSpinCap = 1 << 16;
constexpr unsigned kOut = 0x80000000u;

#define STAMP(i)
#define STAMP_ARG
#define STAMP_PASS
#define STAMP_INIT()
#define STAMP_OUT()
#define CSTAMP(i)
#define CS(set, i)

#define PIN() __builtin_amdgcn_sched_barrier(0)
#define OPAQUE(x) asm volatile("" : "+v"(x))
#define OPAQUE_S(x) asm volatile("" : "+s"(x))
#define NOP(n) asm volatile("s_nop %0" ::"i"(n))
#define WAIT_VM(n) asm volatile("s_waitcnt vmcnt(%0)" ::"i"(n) : "memory")
#define WAIT_LGKM0() do { asm volatile("" ::: "memory"); __builtin_amdgcn_s_waitcnt(0xC07F); asm volatile("" ::: "memory"); } while (0)
#define BARRIER() do { asm volatile("" ::: "memory"); __builtin_amdgcn_s_barrier(); asm volatile("" ::: "memory"); } while (0)

DEV u32x4 make_srd(const char* p, unsigned bytes) { return u32x4{(unsigned)(u64)p, (unsigned)((u64)p >> 32) & 0xffffu, bytes, 0x00020000u}; }
DEV void set_m0(unsigned a, unsigned b) {
  if (__builtin_constant_p(a)) asm volatile("s_add_u32 m0, %1, %0" ::"i"(a), "s"(b) : "scc");
  else asm volatile("s_add_u32 m0, %0, %1" ::"s"(a), "s"(b) : "scc");
}
DEV void dma(unsigned voff, u32x4 srd, unsigned soff, int ofs) {
  asm volatile("buffer_load_dwordx4 %0, %1, %2 offen offset:%3 lds" ::"v"(voff), "s"(srd), "s"(soff), "i"(ofs) : "memory");
}
DEV void dma_part(unsigned voff, u32x4 srd, unsigned soff, u64 lanes) {
  asm volatile("s_mov_b64 exec, %3\n\tbuffer_load_dwordx4 %0, %1, %2 offen lds\n\ts_mov_b64 exec, -1" ::"v"(voff), "s"(srd), "s"(soff), "s"(lanes) : "memory");
}
DEV void dma_dev(unsigned voff, const char* sbase, unsigned dst, u64 lanes) {
  asm volatile("s_mov_b32 m0, %2\n\ts_mov_b64 exec, %3\n\tglobal_load_lds_dwordx4 %0, %1 sc1\n\ts_mov_b64 exec, -1" ::"v"(voff), "s"(sbase), "s"(dst), "s"(lanes) : "memory");
}
DEV void touch(unsigned voff, const char* sbase, unsigned dst, u64 lanes) {
  asm volatile("s_mov_b32 m0, %2\n\ts_mov_b64 exec, %3\n\tglobal_load_lds_dword %0, %1\n\ts_mov_b64 exec, -1" ::"v"(voff), "s"(sbase), "s"(dst), "s"(lanes) : "memory");
}
DEV void store_wt(unsigned voff, u32x4 srd, unsigned soff, int ofs, u32x4 v) {
  asm volatile("buffer_store_dwordx4 %0, %1, %2, %3 offen offset:%4 sc1\n\ts_nop 1" ::"v"(v), "v"(voff), "s"(srd), "s"(soff), "i"(ofs) : "memory");
}
DEV void store_wt_lane0(unsigned voff, const char* sbase, u32x4 v) {
  asm volatile("s_mov_b64 exec, 1\n\tglobal_store_dwordx4 %0, %1, %2 sc1\n\ts_mov_b64 exec, -1\n\ts_nop 0" ::"v"(voff), "v"(v), "s"(sbase) : "memory");
}
DEV void store_flag(unsigned voff, const char* sbase, u64 v) {
  asm volatile("s_mov_b64 exec, 1\n\tglobal_store_dwordx2 %0, %1, %2 sc1\n\ts_mov_b64 exec, -1" ::"v"(voff), "v"(v), "s"(sbase) : "memory");
}

DEV bf16x8 lds_b128(unsigned a) { return *(LAS bf16x8*)(size_t)a; }
DEV f32x4 lds_f4(unsigned a) { return *(LAS f32x4*)(size_t)a; }
DEV bf16x8 lds_tr(unsigned a0, unsigned a1) {
  bf16x4 a = __builtin_amdgcn_ds_read_tr16_b64_v4bf16((LAS bf16x4*)(size_t)a0);
  bf16x4 b = __builtin_amdgcn_ds_read_tr16_b64_v4bf16((LAS bf16x4*)(size_t)a1);
  return __builtin_shufflevector(a, b, 0, 1, 2, 3, 4, 5, 6, 7);
}
DEV f32x4 mfma(bf16x8 a, bf16x8 b, f32x4 c) { return __builtin_amdgcn_mfma_f32_16x16x32_bf16(a, b, c, 0, 0, 0); }
template <class A, class B> DEV void mma(f32x4& c, A a, B b, bool first) {
  if (first) asm volatile("v_mfma_f32_16x16x32_bf16 %0, %1, %2, 0" : "=&v"(c) : "v"(a), "v"(b));
  else asm volatile("v_mfma_f32_16x16x32_bf16 %0, %1, %2, %0" : "+v"(c) : "v"(a), "v"(b));
}
DEV unsigned pack2(f32x2 v) { return __builtin_bit_cast(unsigned, __builtin_convertvector(v, bf16x2)); }
DEV float blo(unsigned u) { return __uint_as_float(u << 16); }
DEV float bhi(unsigned u) { return __uint_as_float(u & 0xffff0000u); }
DEV f32x2 lo2(f32x4 v) { return __builtin_shufflevector(v, v, 0, 1); }
DEV f32x2 hi2(f32x4 v) { return __builtin_shufflevector(v, v, 2, 3); }
DEV f32x2 fma2(float a, f32x2 b, f32x2 c) { return __builtin_elementwise_fma(f32x2{a, a}, b, c); }
template <int C> DEV float dpp(float x) { return __builtin_bit_cast(float, __builtin_amdgcn_update_dpp(0, __builtin_bit_cast(int, x), C, 0xF, 0xF, true)); }
DEV float max3(float a, float b, float c) { float r; asm("v_max3_f32 %0, %1, %2, %3" : "=v"(r) : "v"(a), "v"(b), "v"(c)); return r; }
template <class F> DEV void rows2(float& a, float& b, F f) {
  auto r = __builtin_amdgcn_permlane16_swap(__float_as_uint(a), __float_as_uint(b), false, false);
  const unsigned x = __float_as_uint(f(__uint_as_float(r[0]), __uint_as_float(r[1])));
  r = __builtin_amdgcn_permlane32_swap(x, x, false, false);
  const unsigned y = __float_as_uint(f(__uint_as_float(r[0]), __uint_as_float(r[1])));
  r = __builtin_amdgcn_permlane16_swap(y, y, false, false);
  a = __uint_as_float(r[0]); b = __uint_as_float(r[1]);
}
DEV int mod7(int v) { return v >= 7 ? v - 7 : v; }
constexpr int slot_of(int s) { return (s + 2) % 3 * kSlot; }

struct Stg {
  unsigned vo[6], dx, dp, dw;
  u32x4 xs, ws;
  unsigned xo, wo;
  int ks, kstep;
  DEV void next() { xo = ks * 128; wo = ks * kstep; ks = ks == 6 ? 0 : ks + 1; }
};
struct P2 {
  unsigned va[4], vpad;
  unsigned aw[3][4];
  unsigned sA0, sA2;
  u32x4 s2, so, sa;
  int rot;
  bf16x8 Wf[5][4];
  unsigned flag;
  unsigned fofs;
  DEV void peek(const char* ws) const { dma_dev(fofs, ws, kScr + 1408 + (sA0 >> 12) * kScrW, 3); }
  const char *w2, *wo;
  int rg, ct;
  bool live;
  DEV void init(int lane, int w, const char* ws) {
    const int r7 = lane >> 3;
    const unsigned v0 = r7 * 896 + (((lane & 7) ^ (r7 & 2)) << 4);
#pragma unroll
    for (int i = 0; i < 4; ++i) { va[i] = (v0 ^ ((i & 1) << 6)) + i * (8 * 896 - 1024); OPAQUE(va[i]); }
    s2 = make_srd(w2 + w * (224 * 896) + ct * 128, live ? 224 * 896 : 0);
    sa = make_srd(ws + kWsA2, 32 * 72 * 4096);
    OPAQUE_S(s2); OPAQUE_S(sa);
  }
  DEV void init2(int lane, int w, unsigned own_flag) {
    const int g = lane >> 4, q = (lane >> 2) & 3, p = lane & 3;
    const unsigned a0 = (8 * g + q) * 128 + (p >> 1) * 16 + (p & 1) * 8 + sA0, sx = 2 * (g & 1) + (q >> 1);
#pragma unroll
    for (int i = 0; i < 4; ++i) {
      aw[0][i] = a0 + (i ^ sx) * 32; aw[1][i] = aw[0][i] + (sA2 - sA0); aw[2][i] = aw[0][i] + 98304;
      OPAQUE(aw[0][i]); OPAQUE(aw[1][i]); OPAQUE(aw[2][i]);
    }
    vpad = lane < 32 ? va[3] : kOut;
    fofs = kWsFlags + (((rg >> 1) * 16 + w) * 2 + (rg & 1)) * 128 + (lane & 1) * 16;
    flag = own_flag;
    OPAQUE(vpad); OPAQUE(fofs); OPAQUE(flag);
    so = make_srd(wo + w * (56 * 896) + ct * 128, live ? 56 * 896 : 0);
    OPAQUE_S(so);
    PIN();
  }
  DEV int kstep(int s) const { return mod7(s + rot); }
  DEV void m0(int s) const { if (s == 2 || s == 6) set_m0(0, sA2); else set_m0(s == 5 ? 98304 : s == 1 || s == 4 || s == 8 ? kSlot : 0, sA0); }
  DEV void go(int s, int i) const {
    if (s < 7) dma(va[i], s2, kstep(s) * (32 * 896), i * 1024);
    else dma(i == 3 ? vpad : va[i], so, (s - 7) * (28 * 896), i * 1024);
  }
  DEV void ld(int s) const {
    m0(s); NOP(0);
#pragma unroll
    for (int i = 0; i < 4; ++i) go(s, i);
  }
  DEV void rd1(int s, int u) {
    const unsigned a = s == 2 || s == 6 ? aw[1][u] : s == 5 ? aw[2][u] : aw[0][u] + (s == 1 || s == 4 || s == 8 ? kSlot : 0);
    Wf[s % 5][u] = lds_tr(a, a + 512);
  }
  DEV void rd(int s) {
#pragma unroll
    for (int u = 0; u < 4; ++u) rd1(s, u);
  }
};

template <bool FFN, int NT>
DEV void phase1(Stg& sg, const char* b1, char* ws, u64 tag64, int lane, int w, int rt, int cg, P2& p2 STAMP_ARG) {
  constexpr int WOFF = FFN ? 16384 : 18432, WROW = FFN ? 448 : 384;
  const int mh = w & 1, m = lane & 15, g = lane >> 4, q = m >> 2, p = lane & 3;
  const int t0 = FFN ? (NT == 4 ? (w >> 1) * 4 : 8 + ((w >> 1) - 2) * 3) : (w >> 1) * 3;
  auto tile = [&](int i) { return t0 + (NT == 4 ? i ^ (2 * mh) : i == 0 ? 2 * mh : i == 1 ? 1 - mh : 2 - mh); };
  const int r0 = rt * 128;
  auto ld_m0 = [&](int l, unsigned slot) { if (l == 0 || l == 2 || l == 3) set_m0(slot, l == 0 ? sg.dx : l == 2 ? sg.dp : sg.dw); };
  auto ld_go = [&](int l) {
    if (l < 2) dma(sg.vo[l], sg.xs, sg.xo, l * 1024);
    else if (l == 2) dma_part(sg.vo[2], FFN ? sg.ws : sg.xs, FFN ? sg.wo : sg.xo, FFN ? 0xffffffffull : 0xffffull);
    else dma(sg.vo[l], sg.ws, sg.wo, (l - 3) * 1024);
  };
  unsigned ax[2], ae[2], aw[NT];
#pragma unroll
  for (int kk = 0; kk < 2; ++kk) { ax[kk] = slot_of(0) + (64 * mh + m) * 128 + (((4 * kk + g) ^ (m >> 1)) << 4); ae[kk] = ax[kk] + (8 - 4 * mh) * 2048; }
#pragma unroll
  for (int i = 0; i < NT; ++i)
    aw[i] = slot_of(0) + WOFF + (8 * g + q) * WROW + (p >> 1) * 16 + (p & 1) * 8 + (tile(i) ^ (FFN ? g & 1 : 2 * (g & 1) + (q >> 1))) * 32;
  const unsigned scr = kScr + w * kScrW;
  unsigned ast, acx[NT], abi[NT], sto, sso[NT];
  unsigned co[3], ce[2], ak0, ak1, av0, av1, oso;
  f32x2 kb[4];
  u32x4 one = {0x3f803f80u, 0x3f803f80u, 0x3f803f80u, 0x3f803f80u};
  OPAQUE(one);
  const char* first;
  auto setup = [&]() {
    p2.init(lane, w, ws);
    ast = scr + m * 32; OPAQUE(ast);
#pragma unroll
    for (int n = 0; n < NT; ++n) { acx[n] = kCex + (tile(n) * 16 + 4 * g) * 4; OPAQUE(acx[n]); }
    if (FFN) {
      sto = (g & 1) * 1024 + ((g >> 1) * 16 + m) * 16; OPAQUE(sto);
#pragma unroll
      for (int n = 0; n < NT; ++n) {
        abi[n] = scr + 1024 + (tile(n) - t0) * 32 + g * 8; OPAQUE(abi[n]);
        sso[n] = ((2 * rt + mh) * 72 + 7 * cg + (tile(n) >> 1)) * 4096 + (tile(n) & 1) * 512; OPAQUE_S(sso[n]);
      }
      const int l0 = ((rt * 8 + cg) * 8 + w) * 16;
      first = (l0 < 12544 ? p2.w2 : p2.wo) + (l0 < 12544 ? l0 : l0 < 15680 ? l0 - 12544 : 0) * 128;
      OPAQUE_S(first);
    }
    PIN();
  };
  auto prep = [&](int i) {
    if (FFN) return;
    const int blk = w >> 1, par = w & 1;
    const unsigned rb = kAtt + (par * 72 + 16 * blk) * 400;
    if (i < 3) {
      const int T = tile(i), n = 16 * (T & 3) + 4 * g;
      const unsigned c = kAtt + (T >> 2) * 128 + (n + (n < 28 ? 0 : n < 56 ? 4 : n < 60 ? -28 : 0)) * 2;
      co[i] = c + (8 + 32 * mh + m) * 400; OPAQUE(co[i]);
      if (i < 2) { ce[i] = c + ((m & 1) * 72 + (m >> 1)) * 400; OPAQUE(ce[i]); }
    } else if (i == 3) { ak0 = rb + m * 400 + 16 * g; ak1 = rb + (16 + (m & 7)) * 400 + 128 + 16 * g; OPAQUE(ak0); OPAQUE(ak1); }
    else if (i == 4) { av0 = rb + (4 * g + q) * 400 + 256 + 16 * p; av1 = rb + (16 + 4 * (g & 1) + q) * 400 + 256 + 16 * p; OPAQUE(av0); OPAQUE(av1); }
    else if (i < 9) {
      const int k = i - 5, d = m + 8 - 4 * g - (16 * (k >> 1) + 2 * (k & 1));
      kb[k] = f32x2{(unsigned)d < 8u ? 0.f : -__builtin_inff(), (unsigned)(d - 1) < 8u ? 0.f : -__builtin_inff()};
      OPAQUE(kb[k]);
    }
  };
  oso = (((2 * rt + (w >> 2)) * 72 + 56 + 2 * cg) * 4 + (w & 3)) * 1024;
  const unsigned tag = (unsigned)tag64;
  f32x4 acc[4][NT], ace[2];
  bf16x8 X[2][4], W[2][NT], XE[2];
  constexpr int NRD = NT + 2 + (FFN ? 0 : 1);
  auto rd1 = [&](int kk, int i) {
    if (i == 0) { X[kk][0] = lds_b128(ax[kk]); X[kk][1] = lds_b128(ax[kk] + 2048); }
    else if (i == 2) { X[kk][2] = lds_b128(ax[kk] + 4096); X[kk][3] = lds_b128(ax[kk] + 6144); }
    else if (i < NT + 2) { const int n = i == 1 ? 0 : i - 2; W[kk][n] = lds_tr(aw[n] + kk * 32 * WROW, aw[n] + (kk * 32 + 4) * WROW); }
    else if (i < NRD) XE[kk] = lds_b128(ae[kk]);
  };
  auto half = [&](int kk, bool first, int nex, bool reads, int adv, auto load_at, unsigned slot, auto hook) {
    u32x4 e = one;
#pragma unroll
    for (int i = 0; i < 4 * NT + 4; ++i) {
      if (i >= 4 * NT + nex) break;
      const int l = load_at(i);
      if (l >= 100) { if ((l & 3) == 0) p2.m0((l - 100) >> 2); } else if (l >= 0) ld_m0(l, slot);
      if (!FFN && i == 4 * NT + 2) { e = m == 0 ? one : __builtin_bit_cast(u32x4, XE[1 - kk]); OPAQUE(e); NOP(1); }
      if (i < 4 * NT) mma(acc[i & 3][i >> 2], W[kk][i >> 2], X[kk][i & 3], first);
      else mma(ace[i & 1], W[i < 4 * NT + 2 ? kk : 1 - kk][i & 1], e, first);
      if (l >= 100) p2.go((l - 100) >> 2, l & 3); else if (l >= 0) ld_go(l);
      if (reads && i % 2 == 0 && i / 2 < NRD) {
        const int r = i / 2;
        if (adv) {
          if (r == 0) { ax[0] += adv; OPAQUE(ax[0]); }
          else if (r == 2) { ax[1] += adv; OPAQUE(ax[1]); }
          else if (r < NT + 2) { const int n = r == 1 ? 0 : r - 2; aw[n] += adv; OPAQUE(aw[n]); }
          else { ae[0] += adv; ae[1] += adv; OPAQUE(ae[0]); OPAQUE(ae[1]); }
        }
        rd1(1 - kk, r);
      }
      if (!FFN && nex && i == 4 * NT - 3) { e = m == 0 ? one : __builtin_bit_cast(u32x4, XE[kk]); OPAQUE(e); }
      hook(i);
      PIN();
    }
  };
  auto ldstats = [&](int i) {
    if (i == 0) dma_dev(lane * 16, ws + kWsStats + (r0 + 64 * mh) * 16, scr, ~0ull);
    else if (FFN) dma_dev(lane * 16, b1 + (cg * 14 + t0) * 32, scr + 1024, (1ull << (2 * NT)) - 1);
    else dma_dev(rt == 0 ? 0 : lane * 16, ws + kWsStats + (rt == 0 ? 0 : r0 - 16) * 16, scr + 1024, 0xffffull);
  };
  u32x4 st[5];
  u32x2 bias[NT];
  f32x4 cs[NT];
  unsigned bad = 0;
  auto rdstats = [&](int i) { st[i] = *(LAS u32x4*)(size_t)(i < 4 ? ast + (i >> 1) * 512 + (i & 1) * 16 : scr + 1024 + m * 16); };
  auto check = [&](int i) { bad |= (st[i][1] ^ tag) | (st[i][3] ^ tag); OPAQUE(bad); };
  constexpr int NST = FFN ? 4 : 5;

  WAIT_VM(8);
  CS(2, 4);
  BARRIER();
  STAMP(1);
  CSTAMP(0);
  CS(2, 5);
#pragma unroll
  for (int i = 0; i < NRD; ++i) rd1(0, i);
  sg.next();
#pragma unroll
  for (int l = 0; l < 3; ++l) { ld_m0(l, slot_of(2)); NOP(0); ld_go(l); }
  PIN();
#pragma unroll
  for (int s = 0; s < 6; ++s) {
    auto three = [](int l0) { return [=](int i) { return i % 4 == 1 && i < 12 ? l0 + i / 4 : -1; }; };
    auto four = [](int l0) { return [=](int i) { return i % 3 == 1 && i < 12 ? l0 + i / 3 : -1; }; };
    auto none = [](int) { return -1; };
    if (s < 5) half(0, s == 0, 2, true, 0, three(3), slot_of(s + 2), [&](int i) { if (s == 3 && (i == 11 || i == 13)) ldstats(i == 13); });
    else half(0, false, 2, true, 0, none, 0, [&](int i) {
      if ((i & 1) && i / 2 < NST) rdstats(i / 2);
      if (i >= 9 && i < 9 + NST) check(i - 9);
    });
    if (s == 0) setup();
    if (FFN && s == 1) touch(lane * 128, first, scr + 1280, 0xffff);
    if (s == 5 && __builtin_expect(__ballot(bad != 0) != 0, 0)) {
#pragma nounroll
      for (int spin = 0; spin < kSpinCap; ++spin) {
        __builtin_amdgcn_s_sleep(2);
        ldstats(0);
        if (!FFN) ldstats(1);
        WAIT_VM(0);
        bad = 0;
#pragma unroll
        for (int i = 0; i < NST; ++i) { rdstats(i); check(i); }
        if (__ballot(bad != 0) == 0) break;
      }
    }
    WAIT_LGKM0();
    if (s < 5) WAIT_VM(6 + (FFN && s == 1) + (s == 3 ? 2 : 0));
    else WAIT_VM(4);
    BARRIER();
    if (s < 6) CS(6, s < 6 ? s : 0);
    if (s >= 1) CS(7, s >= 1 ? s - 1 : 0);
    if (s < 4) sg.next();
    PIN();
    if (s < 4) half(1, false, 2, true, slot_of(s + 1) - slot_of(s), three(0), slot_of(s), [&](int i) { if (s >= 1 && i % 4 == 3) prep(3 * (s - 1) + i / 4); });
    else half(1, false, 2, true, slot_of(s + 1) - slot_of(s), four(100 + 4 * (s - 4)), 0, [&](int) {});
  }
  CSTAMP(2);
  auto blk4 = [&](int kk, int n, auto hook) {
#pragma unroll
    for (int mt = 0; mt < 4; ++mt) { mma(acc[mt][n], W[kk][n], X[kk][mt], false); hook(mt); PIN(); }
  };
  {
    u32x4 e0 = one, e1 = one;
    blk4(0, 0, [&](int i) { rd1(1, i == 0 ? 1 : i == 1 ? 3 : i == 2 ? (FFN ? 0 : NRD - 1) : (FFN ? 2 : 0)); });
    if (!FFN) { e0 = m == 0 ? one : __builtin_bit_cast(u32x4, XE[0]); OPAQUE(e0); }
    blk4(0, 1, [&](int i) {
      if (FFN) { if (i + 4 < NRD) rd1(1, i + 4); bias[i < NT ? i : 0] = *(LAS u32x2*)(size_t)abi[i < NT ? i : 0]; }
      else if (i < 2) rd1(1, i == 0 ? 2 : 4);
    });
    mma(ace[0], W[0][0], e0, false); mma(ace[1], W[0][1], e0, false);
    if (!FFN) { e1 = m == 0 ? one : __builtin_bit_cast(u32x4, XE[1]); OPAQUE(e1); NOP(1); }
    mma(ace[0], W[1][0], e1, false); mma(ace[1], W[1][1], e1, false);
    PIN();
    blk4(0, 2, [&](int) {});
    NOP(3);
    asm volatile("s_mov_b64 exec, %4\n\tds_write_b128 %0, %1\n\tds_write_b128 %2, %3\n\ts_mov_b64 exec, -1\n\ts_waitcnt lgkmcnt(0)"
                 ::"v"(acx[0]), "v"(ace[0]), "v"(acx[1]), "v"(ace[1]), "s"(0x0001000100010001ull) : "memory");
  }
  WAIT_LGKM0();
  BARRIER();
  CS(7, 5);
  CSTAMP(3);
  if (PRIO & 8) asm volatile("s_cmp_lt_u32 %0, 4\n\ts_cbranch_scc1 1f\n\ts_setprio 1\n1:" ::"s"(w) : "scc");
  p2.m0(2);
  blk4(1, 0, [&](int i) { p2.go(2, i); if (i < NT) cs[i] = lds_f4(acx[i]); });
  blk4(1, 1, [&](int) {});
  float rs[5], mu[5];
#pragma unroll
  for (int i = 0; i < NST; ++i) { rs[i] = __uint_as_float(st[i][2]); mu[i] = FFN ? -rs[i] * __uint_as_float(st[i][0]) : -__uint_as_float(st[i][0]); }
  auto epi = [&](int n) {
    if (FFN) {
      const f32x2 b0 = {blo(bias[n][0]), bhi(bias[n][0])}, b1v = {blo(bias[n][1]), bhi(bias[n][1])};
#pragma unroll
      for (int pp = 0; pp < 2; ++pp) {
        unsigned y[2][2];
#pragma unroll
        for (int e = 0; e < 2; ++e) {
          const int mt = 2 * pp + e;
          f32x2 u0 = fma2(rs[mt], lo2(acc[mt][n]), fma2(mu[mt], lo2(cs[n]), b0)), u1 = fma2(rs[mt], hi2(acc[mt][n]), fma2(mu[mt], hi2(cs[n]), b1v));
          u0 = f32x2{fmaxf(u0[0], 0.f), fmaxf(u0[1], 0.f)}; u1 = f32x2{fmaxf(u1[0], 0.f), fmaxf(u1[1], 0.f)};
          y[e][0] = pack2(u0 * u0); y[e][1] = pack2(u1 * u1);
        }
        const auto s0 = __builtin_amdgcn_permlane16_swap(y[0][0], y[1][0], false, false);
        const auto s1 = __builtin_amdgcn_permlane16_swap(y[0][1], y[1][1], false, false);
        store_wt(sto, p2.sa, sso[n], pp * 2048, u32x4{s0[0], s1[0], s0[1], s1[1]});
      }
    } else {
      auto put = [&](const f32x4& a, int i, unsigned addr) {
        *(LAS u32x2*)(size_t)addr = u32x2{pack2(fma2(mu[i], lo2(cs[n]), lo2(a)) * rs[i]), pack2(fma2(mu[i], hi2(cs[n]), hi2(a)) * rs[i])};
      };
      if (n < 2) put(ace[n], 4, ce[n]);
#pragma unroll
      for (int mt = 0; mt < 4; ++mt) put(acc[mt][n], mt, co[n] + ((mt & 1) * 72 + 16 * (mt >> 1)) * 400);
    }
    PIN();
  };
  PIN();
  NOP(3);
  PIN();
  epi(0);
  blk4(1, 2, [&](int) {});
  epi(1);
  if (NT == 4) { blk4(0, 3, [&](int) {}); blk4(1, 3, [&](int) {}); }
  epi(2);
  if (NT == 4) epi(3);
  CSTAMP(4);
  CS(3, 0);
  CS(4, 0);
  STAMP(2);
  p2.init2(lane, w, kWsFlags + ((rt * 16 + cg + (FFN ? 0 : 8)) * 2 + (FFN ? w & 1 : w >> 2)) * 128 + (FFN ? w >> 1 : w & 3) * 8);
  if (FFN) {
    STAMP(3);
    CS(3, 2);
    WAIT_VM(0);
    store_flag(p2.flag, ws, tag64);
    STAMP(4);
    CS(3, 3);
    p2.rd(0);
    p2.rd(1);
    p2.rd(2);
    WAIT_LGKM0();
    BARRIER();
    CS(3, 4);
    p2.ld(3); p2.ld(4); p2.ld(5);
    if (LATE78) { p2.peek(ws); p2.ld(6); }
    else {
      p2.ld(6);
      WAIT_VM(12); p2.rd(3); WAIT_LGKM0();
      p2.ld(7);
      WAIT_VM(12); p2.rd(4);
      p2.peek(ws);
      WAIT_LGKM0();
      p2.ld(8);
    }
    CS(3, 5);
  } else {
    WAIT_VM(4);
    p2.rd(0);
    p2.rd(1);
    WAIT_LGKM0();
    CS(4, 1);
    BARRIER();
    CS(4, 2);
    p2.ld(3);
    const f32x4 z = {0.f, 0.f, 0.f, 0.f};
    f32x4 sc[2][2];
    bf16x8 qf[2], kf[2][2], vf[2][2];
#pragma unroll
    for (int hd = 0; hd < 2; ++hd) { qf[hd] = lds_b128(ak0 + 8 * 400 + hd * 64); kf[hd][0] = lds_b128(ak0 + 128 + hd * 64); kf[hd][1] = lds_b128(ak1 + hd * 64); }
#pragma unroll
    for (int hd = 0; hd < 2; ++hd) { vf[hd][0] = lds_tr(av0 + hd * 64, av1 + hd * 64); vf[hd][1] = lds_tr(av0 + hd * 64 + 8, av1 + hd * 64 + 8); }
    PIN();
#pragma unroll
    for (int hd = 0; hd < 2; ++hd) { sc[hd][0] = mfma(kf[hd][0], qf[hd], z); sc[hd][1] = mfma(kf[hd][1], qf[hd], z); }
    PIN();
    p2.ld(4);
    WAIT_VM(8);
    p2.rd(2);
    WAIT_LGKM0();
    BARRIER();
    CS(4, 3);
    p2.ld(5);
    PIN();
    f32x2 e[2][4];
    float mx[2], sum[2];
    const float kLog = 0.18898223650461362f * 1.4426950408889634f;
#pragma unroll
    for (int hd = 0; hd < 2; ++hd) {
#pragma unroll
      for (int t = 0; t < 2; ++t) {
        e[hd][2 * t] = fma2(kLog, lo2(sc[hd][t]), kb[2 * t]);
        e[hd][2 * t + 1] = fma2(kLog, hi2(sc[hd][t]), kb[2 * t + 1]);
      }
    }
    if (__builtin_expect(r0 + 32 * (w >> 1) == 0, 0)) {
#pragma unroll
      for (int i = 0; i < 2; ++i) {
        const f32x2 v = kb[i] + -1.0e9f * 1.4426950408889634f;
        if (g < 2) e[0][i] = e[1][i] = v;
      }
    }
#pragma unroll
    for (int hd = 0; hd < 2; ++hd) {
      mx[hd] = max3(max3(e[hd][0][0], e[hd][0][1], e[hd][1][0]), max3(e[hd][1][1], e[hd][2][0], e[hd][2][1]), max3(e[hd][3][0], e[hd][3][1], e[hd][3][1]));
    }
    rows2(mx[0], mx[1], [](float a, float b) { return max3(a, b, b); });
    PIN();
    if (!LATE78) p2.ld(6);
    PIN();
#pragma unroll
    for (int hd = 0; hd < 2; ++hd) {
#pragma unroll
      for (int i = 0; i < 4; ++i) { const f32x2 d = e[hd][i] - mx[hd]; e[hd][i] = f32x2{__builtin_amdgcn_exp2f(d[0]), __builtin_amdgcn_exp2f(d[1])}; }
      const f32x2 t = (e[hd][0] + e[hd][1]) + (e[hd][2] + e[hd][3]);
      sum[hd] = t[0] + t[1];
    }
    rows2(sum[0], sum[1], [](float a, float b) { return a + b; });
    f32x4 o[2][2];
#pragma unroll
    for (int hd = 0; hd < 2; ++hd) {
      const float inv = __builtin_amdgcn_rcpf(sum[hd]);
      const u32x4 pf = {pack2(e[hd][0] * inv), pack2(e[hd][1] * inv), pack2(e[hd][2] * inv), pack2(e[hd][3] * inv)};
      o[hd][0] = mfma(vf[hd][0], __builtin_bit_cast(bf16x8, pf), z);
      o[hd][1] = mfma(vf[hd][1], __builtin_bit_cast(bf16x8, pf), z);
    }
    if (!LATE78) { WAIT_VM(8); p2.rd(3); p2.rd(4); }
#pragma unroll
    for (int hd = 0; hd < 2; ++hd)
      store_wt(lane * 16, p2.sa, oso + hd * 4096, 0, u32x4{pack2(lo2(o[hd][0])), pack2(hi2(o[hd][0])), pack2(lo2(o[hd][1])), pack2(hi2(o[hd][1]))});
    STAMP(3);
    CS(4, 4);
    if (LATE78) { p2.peek(ws); p2.ld(6); WAIT_VM(5); store_flag(p2.flag, ws, tag64); }
    else {
      WAIT_LGKM0();
      p2.ld(7);
      WAIT_VM(4);
      store_flag(p2.flag, ws, tag64);
      p2.peek(ws);
      p2.ld(8);
    }
    STAMP(4);
    CS(4, 5);
  }
}

__global__ __launch_bounds__(512, 2) void fused_kernel(const char*, const char*, const char*, const char*, const char*, const char*, const char*, char*, char*) {
  STAMP_INIT();
  STAMP(0);
  u32x16 ka;
  u64 kb;
  asm volatile("s_load_dwordx16 %0, %2, 0x0\n\ts_load_dwordx2 %1, %2, 0x40" : "=&s"(ka), "=&s"(kb) : "s"(__builtin_amdgcn_kernarg_segment_ptr()));
  __shared__ __attribute__((aligned(16))) char smem[kLds];
  asm volatile("" ::"v"((LAS char*)smem));
  const int lane = threadIdx.x & 63, w = __builtin_amdgcn_readfirstlane(threadIdx.x >> 6);
  const int bid = blockIdx.x, xcd = bid & 7, j = bid >> 3;
  if (PRIO & 1) asm volatile("s_cmp_lt_u32 %0, 4\n\ts_cbranch_scc1 1f\n\ts_setprio 1\n1:" ::"s"(w) : "scc");
  const int rt = (xcd & 1) * 8 + (j & 7), cgi = j >> 3, cg = 2 * (xcd >> 1) + (cgi & 1);
  const bool ffn = cgi < 2;
  const char *x, *wo, *b1, *w2, *b2;
  char *out, *ws;
  Stg sg;
  u64 tag64;
  {
    const int r0 = rt * 128;
#pragma unroll
    for (int u = 0; u < 2; ++u) {
      const unsigned r = 8 * u + (lane >> 3);
      sg.vo[u] = __umul24(r0 + 32 * (w >> 1) + 2 * r + (w & 1), 896) + (((lane & 7) ^ (r >> 1)) << 4) - u * 1024;
    }
    sg.dx = w * 2048;
    sg.ks = mod7((rt & 3) * 2 + (xcd >> 1));
    asm volatile("" : "+v"(sg.vo[0]), "+v"(sg.vo[1]), "+s"(sg.ks), "+s"(sg.dx));
    asm volatile("s_waitcnt lgkmcnt(0)" : "+s"(ka), "+s"(kb));
    auto ptr = [&](int i) { return (char*)(((u64)ka[2 * i + 1] << 32) | ka[2 * i]); };
    x = ptr(0); wo = ptr(2); b1 = ptr(4); w2 = ptr(5); b2 = ptr(6); out = ptr(7); ws = (char*)kb;
    CS(2, 0);
    sg.xs = make_srd(x, 2048 * 896);
    set_m0(slot_of(0), sg.dx);
    NOP(0);
    dma(sg.vo[0], sg.xs, sg.ks * 128, 0);
    dma(sg.vo[1], sg.xs, sg.ks * 128, 1024);
    PIN();
    const unsigned q0 = 48 * w + (lane >> 2), l3 = (lane & 3) << 4;
    if (ffn) {
      auto woff = [&](unsigned Q) { const unsigned k = __umul24(Q, 9363) >> 16; return __umul24(k, 3584 - 448) + Q * 64 + cg * 448 + (l3 ^ ((k << 2) & 32)); };
#pragma unroll
      for (int u = 0; u < 3; ++u) sg.vo[3 + u] = woff(q0 + 16 * u) - u * 1024;
      sg.vo[2] = woff(384 + 8 * w + ((lane >> 2) & 7));
    } else {
#pragma unroll
      for (int u = 0; u < 3; ++u) {
        const unsigned Q = q0 + 16 * u, k = __umul24(Q, 10923) >> 16, cq = Q - 6 * k, c = (((cq & 1) << 6) | l3) ^ ((k & 2) << 4) ^ ((k & 8) << 3);
        sg.vo[3 + u] = (c == 112 ? kOut : __umul24(k, 2688) + __umul24(cq >> 1, 896) + cg * 112 + c) - u * 1024;
      }
      const int hr = r0 - 16 + 2 * w + ((lane >> 3) & 1);
      sg.vo[2] = __umul24(hr < 0 ? 0 : hr, 896) + (((lane & 7) ^ w) << 4);
    }
    sg.dw = (ffn ? 16384 : 18432) + w * 3072;
    sg.dp = ffn ? 16384 + 24576 + w * 512 : 16384 + w * 256;
    sg.kstep = ffn ? 64 * 3584 : 64 * 2688;
    sg.ws = make_srd(ffn ? ptr(3) : ptr(1), 7 * sg.kstep);
    const u32x4 ps = ffn ? sg.ws : sg.xs;
    const u64 part = ffn ? 0xffffffffull : 0xffffull;
    auto ld = [&](int s, int l) {
      if (l == 0 || l == 2 || l == 3) set_m0(slot_of(s), l == 0 ? sg.dx : l == 2 ? sg.dp : sg.dw);
      if (l == 0 || l == 3) NOP(0);
      if (l < 2) dma(sg.vo[l], sg.xs, sg.xo, l * 1024);
      else if (l == 2) dma_part(sg.vo[2], ps, ffn ? sg.wo : sg.xo, part);
      else dma(sg.vo[l], sg.ws, sg.wo, (l - 3) * 1024);
    };
    const int strow = (xcd & 1) * 1024 + (xcd >> 1) * 256 + j * 8 + w;
    u32x2 ep;
    u32x4 srow;
    asm volatile("s_mov_b64 exec, 1\n\tglobal_load_dwordx2 %0, %1, %2 sc1\n\ts_mov_b64 exec, -1" : "=v"(ep) : "v"(0), "s"(ws) : "memory");
    asm volatile("buffer_load_dwordx4 %0, %1, %2, %3 offen" : "=v"(srow) : "v"(lane < 56 ? lane * 16 : kOut), "s"(sg.xs), "s"(strow * 896) : "memory");
    sg.next();
    ld(0, 3); ld(0, 4); ld(0, 5); ld(0, 2);
    {
      const char* pc;
      unsigned nl;
      asm volatile("s_getpc_b64 %0\n.Lfk_pc:\n\ts_mov_b32 %1, (.Lfk_end - .Lfk_pc) / 128" : "=s"(pc), "=s"(nl));
      const unsigned li = (j * 8 + w) * 2;
      touch((li + lane) * 128, pc, kScr + w * kScrW + 1280, (1u << (min(nl, li + 2) - min(nl, li))) - 1);
    }
    CS(2, 1);
    asm volatile("s_waitcnt vmcnt(5)" : "+v"(srow), "+v"(ep)::"memory");
    tag64 = (((u64)__builtin_amdgcn_readfirstlane(ep[1]) << 32) | (unsigned)__builtin_amdgcn_readfirstlane(ep[0])) + 1;
    float sm = 0.f, sq = 0.f;
#pragma unroll
    for (int i = 0; i < 4; ++i) {
      const unsigned u = srow[i];
      const bf16x2 v = __builtin_bit_cast(bf16x2, u);
      sm = __builtin_amdgcn_fdot2_f32_bf16(v, __builtin_bit_cast(bf16x2, 0x3f803f80u), sm, false);
      sq = __builtin_amdgcn_fdot2_f32_bf16(v, v, sq, false);
    }
    sm += dpp<0xB1>(sm); sq += dpp<0xB1>(sq);
    sm += dpp<0x4E>(sm); sq += dpp<0x4E>(sq);
    sm += dpp<0x141>(sm); sq += dpp<0x141>(sq);
    sm += dpp<0x140>(sm); sq += dpp<0x140>(sq);
    rows2(sm, sq, [](float a, float b) { return a + b; });
    sm *= 1.f / 448.f;
    store_wt_lane0(kWsStats + strow * 16, ws,
                   u32x4{__float_as_uint(sm), (unsigned)tag64, __float_as_uint(__builtin_amdgcn_rsqf(fmaxf(fmaf(-sm, sm, sq * (1.f / 448.f)), 0.f) + 1e-5f)), (unsigned)tag64});
    CS(2, 3);
    sg.next();
#pragma unroll
    for (int l = 0; l < 6; ++l) ld(1, l);
    CS(2, 2);
  }
  const int rg = 4 * xcd + (j & 3), ct = mod7(mod7(7 + (j < 28 ? j >> 2 : 6) - 2 * (j & 3)));
  const int m = lane & 15, g = lane >> 4;
  P2 p2;
  p2.sA0 = w * 4096; p2.sA2 = (w < 3 ? 32768 : 65536) + w * 4096; p2.rot = 2 * (rg & 3);
  p2.w2 = w2; p2.wo = wo; p2.rg = rg; p2.ct = ct; p2.live = j < 28;

  if (!ffn) phase1<false, 3>(sg, b1, ws, tag64, lane, w, rt, cg, p2 STAMP_PASS);
  else if (w < 4) phase1<true, 4>(sg, b1, ws, tag64, lane, w, rt, cg, p2 STAMP_PASS);
  else phase1<true, 3>(sg, b1, ws, tag64, lane, w, rt, cg, p2 STAMP_PASS);

  if (j >= 28) {
    if (bid == 255) {
      const unsigned t0 = (unsigned)tag64, t1 = (unsigned)(tag64 >> 32);
#pragma nounroll
      for (int spin = 0; spin < kSpinCap; ++spin) {
        u32x4 f, h;
        asm volatile("global_load_dwordx4 %0, %2, %3 sc1\n\tglobal_load_dwordx4 %1, %2, %4 sc1\n\ts_waitcnt vmcnt(0)" : "=&v"(f), "=&v"(h)
                     : "v"((lane >> 1) * 128 + (lane & 1) * 16), "s"(ws + kWsFlags + w * 8192), "s"(ws + kWsFlags + w * 8192 + 4096) : "memory");
        if (__all(f[0] == t0 && f[1] == t1 && f[2] == t0 && f[3] == t1 && h[0] == t0 && h[1] == t1 && h[2] == t0 && h[3] == t1)) break;
        __builtin_amdgcn_s_sleep(8);
      }
      BARRIER();
      if (w == 0) store_flag(0, ws, tag64);
    }
    STAMP(7);
    STAMP_OUT();
    return;
  }

  const unsigned fofs = kWsFlags + (((rg >> 1) * 16 + w) * 2 + (rg & 1)) * 128 + (lane & 3) * 8;
  auto ready = [&](u32x2 f) { return (__ballot(f[0] == (unsigned)tag64 && f[1] == (unsigned)(tag64 >> 32)) & 15) == 15; };
  auto poll = [&](unsigned ofs) {
#pragma nounroll
    for (int spin = 0; spin < kSpinCap; ++spin) {
      u32x2 f;
      asm volatile("s_mov_b64 exec, 15\n\tglobal_load_dwordx2 %0, %1, %2 sc1\n\ts_mov_b64 exec, -1\n\ts_waitcnt vmcnt(0)" : "=&v"(f) : "v"(ofs), "s"(ws) : "memory");
      if (ready(f)) break;
      __builtin_amdgcn_s_sleep(1);
    }
  };
  CS(5, 0);
  WAIT_VM(4);
  if (!ready(*(LAS u32x2*)(size_t)(kScr + 1408 + w * kScrW + (lane & 3) * 8))) poll(fofs);
  STAMP(5);
  u32x4 H[WIN][4];
  auto hso = [&](int i) { return (unsigned)(rg * 72 + (i < 7 ? 7 * w + p2.kstep(i) : 56 + 2 * w + (i - 7))) * 4096; };
  auto ldh = [&](int i, int mt, unsigned so) {
    asm volatile("buffer_load_dwordx4 %0, %1, %2, %3 offen offset:%4 sc1" : "=v"(H[i % WIN][mt]) : "v"(lane * 16), "s"(p2.sa), "s"(so), "i"(mt * 1024) : "memory");
  };
  CS(5, 1);
  if (PRIO & 4) asm volatile("s_cmp_lt_u32 %0, 4\n\ts_cbranch_scc1 1f\n\ts_setprio 1\n1:" ::"s"(w) : "scc");
  CS(10, 0);
#pragma unroll
  for (int i = 0; i < WIN; ++i) {
    const unsigned so = hso(i);
#pragma unroll
    for (int mt = 0; mt < 4; ++mt) ldh(i, mt, so);
  }
  const int os = threadIdx.x >> 3, orow = rg * 64 + 32 * (os >> 5) + 2 * (os & 15) + ((os >> 4) & 1);
  unsigned oofs = __umul24(orow, 896) + ct * 128 + (threadIdx.x & 7) * 16;
  u32x4 xr, br;
  unsigned pa[3][4], ra[2][2];
#pragma unroll
  for (int u = 0; u < 4; ++u) {
    const unsigned a = m * 256 + (((4 * u + g) ^ (m & 7)) << 4);
    pa[0][u] = a + p2.sA0; pa[1][u] = a + p2.sA0 + 98304; pa[2][u] = a + p2.sA2;
    OPAQUE(pa[0][u]); OPAQUE(pa[1][u]); OPAQUE(pa[2][u]);
  }
  ra[0][0] = (w < 2 ? 0 : w < 4 ? kSlot : w < 6 ? 98304 : 32768) + (os & 15) * 256 + (((2 * (threadIdx.x & 7)) ^ (os & 7)) << 4);
  ra[0][1] = ra[0][0] ^ 16;
  ra[1][0] = ra[0][0] + (w < 6 ? 0 : 32768);
  ra[1][1] = ra[1][0] ^ 16;
  OPAQUE(ra[0][0]); OPAQUE(ra[0][1]); OPAQUE(ra[1][0]); OPAQUE(ra[1][1]); OPAQUE(oofs);
  f32x4 acc[4][4], lo, hi;
  u32x2 fq;
  auto park = [&](int mt, int u) { *(LAS f32x4*)(size_t)(mt < 2 ? pa[0][u] + mt * kSlot : pa[mt - 1][u]) = acc[mt][u]; };
#pragma unroll
  for (int i = 0; i < 9; ++i) {
    const unsigned so = i + WIN < 9 ? hso(i + WIN) : 0;
    if (i == 7 - WIN) {
      asm volatile("s_waitcnt vmcnt(6)" : "+v"(fq)::"memory");
      if (!ready(fq)) poll(fofs + 2048);
    }
#pragma unroll
    for (int mt = 0; mt < 4; ++mt) {
      const int left = 4 * (8 - i) + 3 - mt;
      const int younger = (left < 4 * WIN - 1 ? left : 4 * WIN - 1) + (LATE78 && i >= 1 && i <= WIN ? 8 : 0);
      asm volatile("s_waitcnt vmcnt(%1)" : "+v"(H[i % WIN][mt]) : "i"(younger) : "memory");
      if (mt == 0 && i % 2 == 0) CS(10, 1 + i / 2);
#pragma unroll
      for (int u = 0; u < 4; ++u) {
        mma(acc[mt][u], p2.Wf[i % 5][u], H[i % WIN][mt], i == 0);
        if (LATE78 && i == 0 && u < 2) p2.rd1(3 + u, mt);
        if (u == 0 && i >= 1 && i < 3) p2.rd1(i + 4, mt);
        if (u == 0 && i >= 4 && i < 6) p2.rd1(i + 3, mt);
        if (i == 7 && mt == 0 && u == 0) {
          OPAQUE(xr); OPAQUE(br);
          lo = f32x4{blo(xr[0]) + blo(br[0]), bhi(xr[0]) + bhi(br[0]), blo(xr[1]) + blo(br[1]), bhi(xr[1]) + bhi(br[1])};
          hi = f32x4{blo(xr[2]) + blo(br[2]), bhi(xr[2]) + bhi(br[2]), blo(xr[3]) + blo(br[3]), bhi(xr[3]) + bhi(br[3])};
          OPAQUE(lo); OPAQUE(hi);
        }
        if (i == 8 && mt >= 1) park(mt - 1, u);
        PIN();
      }
      if (i + WIN < 9) ldh(i, mt, so);
    }
    if (LATE78 && i == 0) { WAIT_LGKM0(); p2.ld(7); p2.ld(8); }
    if (i == 5 - WIN) asm volatile("s_mov_b64 exec, 15\n\tglobal_load_dwordx2 %0, %1, %2 offset:2048 sc1\n\ts_mov_b64 exec, -1" : "=v"(fq) : "v"(fofs), "s"(ws) : "memory");
    if (i == 6 - WIN) {
      asm volatile("global_load_dwordx4 %0, %1, %2" : "=v"(xr) : "v"(oofs), "s"(x) : "memory");
      asm volatile("global_load_dwordx4 %0, %1, %2" : "=v"(br) : "v"(ct * 128 + (threadIdx.x & 7) * 16), "s"(b2) : "memory");
    }
  }
  CS(5, 2);
  STAMP(6);
  NOP(6);
  PIN();
#pragma unroll
  for (int u = 0; u < 4; ++u) park(3, u);
  WAIT_LGKM0();
  CS(5, 3);
  BARRIER();
  CS(5, 4);
#pragma unroll
  for (int k = 0; k < 8; k += 2) {
    lo += lds_f4(ra[k >= 3][0] + k * 4096) + lds_f4(ra[k + 1 >= 3][0] + (k + 1) * 4096);
    hi += lds_f4(ra[k >= 3][1] + k * 4096) + lds_f4(ra[k + 1 >= 3][1] + (k + 1) * 4096);
  }
  *(__attribute__((address_space(1))) u32x4*)(out + oofs) = u32x4{pack2(lo2(lo)), pack2(hi2(lo)), pack2(lo2(hi)), pack2(hi2(hi))};
  CS(5, 5);
  STAMP(7);
  STAMP_OUT();
  asm volatile(".Lfk_end:");
}

extern "C" void kernel_launch(void* const* d_in, const int* in_sizes, int n_in, void* d_out, int out_size, void* d_ws, size_t ws_size, hipStream_t stream) {
  fused_kernel<<<256, 512, 0, stream>>>((const char*)d_in[0], (const char*)d_in[1], (const char*)d_in[2], (const char*)d_in[4], (const char*)d_in[5],
                                        (const char*)d_in[6], (const char*)d_in[7], (char*)d_out, (char*)d_ws);
}
```

```cpp
#include <hip/hip_runtime.h>
#include <hip/hip_bf16.h>

#define LATE78 0
#define WIN 2
#define PRIO 4
typedef __bf16 bf16x2 __attribute__((ext_vector_type(2)));
typedef __bf16 bf16x4 __attribute__((ext_vector_type(4)));
typedef __bf16 bf16x8 __attribute__((ext_vector_type(8)));
typedef float f32x2 __attribute__((ext_vector_type(2)));
typedef float f32x4 __attribute__((ext_vector_type(4)));
typedef unsigned u32x2 __attribute__((ext_vector_type(2)));
typedef unsigned u32x4 __attribute__((ext_vector_type(4)));
typedef unsigned u32x16 __attribute__((ext_vector_type(16)));
typedef unsigned long long u64;
#define LAS __attribute__((address_space(3)))
#define DEV __device__ __forceinline__

constexpr int kLds = 163840;
constexpr int kSlot = 45056;
constexpr int kCex = kLds - 1024;
constexpr int kAtt = kCex - 144 * 400;
constexpr int kScr = 3 * kSlot, kScrW = 1536;
constexpr int kWsFlags = 4096, kWsStats = 131072, kWsA2 = 262144, kWsDiag = 16 << 20;
constexpr int kSpinCap = 1 << 16;
constexpr unsigned kOut = 0x80000000u;

#define PROBE_K 0
#define STAMP(i) do { if ((i) == PROBE_K) asm volatile("s_memrealtime %0\n\ts_waitcnt lgkmcnt(0)" : "=s"(tstamp)::"memory"); } while (0)
#define STAMP_ARG , u64& tstamp
#define STAMP_PASS , tstamp
#define STAMP_INIT() u64 tstamp = 0
#define STAMP_OUT() do { if (tstamp) { u64 n_; do { __builtin_amdgcn_s_sleep(4); asm volatile("s_memrealtime %0\n\ts_waitcnt lgkmcnt(0)" : "=s"(n_)::"memory"); } while (n_ < tstamp + 3000); } } while (0)
#define CSTAMP(i)
#define CS(set, i)

#define PIN() __builtin_amdgcn_sched_barrier(0)
#define OPAQUE(x) asm volatile("" : "+v"(x))
#define OPAQUE_S(x) asm volatile("" : "+s"(x))
#define NOP(n) asm volatile("s_nop %0" ::"i"(n))
#define WAIT_VM(n) asm volatile("s_waitcnt vmcnt(%0)" ::"i"(n) : "memory")
#define WAIT_LGKM0() do { asm volatile("" ::: "memory"); __builtin_amdgcn_s_waitcnt(0xC07F); asm volatile("" ::: "memory"); } while (0)
#define BARRIER() do { asm volatile("" ::: "memory"); __builtin_amdgcn_s_barrier(); asm volatile("" ::: "memory"); } while (0)

DEV u32x4 make_srd(const char* p, unsigned bytes) { return u32x4{(unsigned)(u64)p, (unsigned)((u64)p >> 32) & 0xffffu, bytes, 0x00020000u}; }
DEV void set_m0(unsigned a, unsigned b) {
  if (__builtin_constant_p(a)) asm volatile("s_add_u32 m0, %1, %0" ::"i"(a), "s"(b) : "scc");
  else asm volatile("s_add_u32 m0, %0, %1" ::"s"(a), "s"(b) : "scc");
}
DEV void dma(unsigned voff, u32x4 srd, unsigned soff, int ofs) {
  asm volatile("buffer_load_dwordx4 %0, %1, %2 offen offset:%3 lds" ::"v"(voff), "s"(srd), "s"(soff), "i"(ofs) : "memory");
}
DEV void dma_part(unsigned voff, u32x4 srd, unsigned soff, u64 lanes) {
  asm volatile("s_mov_b64 exec, %3\n\tbuffer_load_dwordx4 %0, %1, %2 offen lds\n\ts_mov_b64 exec, -1" ::"v"(voff), "s"(srd), "s"(soff), "s"(lanes) : "memory");
}
DEV void dma_dev(unsigned voff, const char* sbase, unsigned dst, u64 lanes) {
  asm volatile("s_mov_b32 m0, %2\n\ts_mov_b64 exec, %3\n\tglobal_load_lds_dwordx4 %0, %1 sc1\n\ts_mov_b64 exec, -1" ::"v"(voff), "s"(sbase), "s"(dst), "s"(lanes) : "memory");
}
DEV void touch(unsigned voff, const char* sbase, unsigned dst, u64 lanes) {
  asm volatile("s_mov_b32 m0, %2\n\ts_mov_b64 exec, %3\n\tglobal_load_lds_dword %0, %1\n\ts_mov_b64 exec, -1" ::"v"(voff), "s"(sbase), "s"(dst), "s"(lanes) : "memory");
}
DEV void store_wt(unsigned voff, u32x4 srd, unsigned soff, int ofs, u32x4 v) {
  asm volatile("buffer_store_dwordx4 %0, %1, %2, %3 offen offset:%4 sc1\n\ts_nop 1" ::"v"(v), "v"(voff), "s"(srd), "s"(soff), "i"(ofs) : "memory");
}
DEV void store_wt_lane0(unsigned voff, const char* sbase, u32x4 v) {
  asm volatile("s_mov_b64 exec, 1\n\tglobal_store_dwordx4 %0, %1, %2 sc1\n\ts_mov_b64 exec, -1\n\ts_nop 0" ::"v"(voff), "v"(v), "s"(sbase) : "memory");
}
DEV void store_flag(unsigned voff, const char* sbase, u64 v) {
  asm volatile("s_mov_b64 exec, 1\n\tglobal_store_dwordx2 %0, %1, %2 sc1\n\ts_mov_b64 exec, -1" ::"v"(voff), "v"(v), "s"(sbase) : "memory");
}

DEV bf16x8 lds_b128(unsigned a) { return *(LAS bf16x8*)(size_t)a; }
DEV f32x4 lds_f4(unsigned a) { return *(LAS f32x4*)(size_t)a; }
DEV bf16x8 lds_tr(unsigned a0, unsigned a1) {
  bf16x4 a = __builtin_amdgcn_ds_read_tr16_b64_v4bf16((LAS bf16x4*)(size_t)a0);
  bf16x4 b = __builtin_amdgcn_ds_read_tr16_b64_v4bf16((LAS bf16x4*)(size_t)a1);
  return __builtin_shufflevector(a, b, 0, 1, 2, 3, 4, 5, 6, 7);
}
DEV f32x4 mfma(bf16x8 a, bf16x8 b, f32x4 c) { return __builtin_amdgcn_mfma_f32_16x16x32_bf16(a, b, c, 0, 0, 0); }
template <class A, class B> DEV void mma(f32x4& c, A a, B b, bool first) {
  if (first) asm volatile("v_mfma_f32_16x16x32_bf16 %0, %1, %2, 0" : "=&v"(c) : "v"(a), "v"(b));
  else asm volatile("v_mfma_f32_16x16x32_bf16 %0, %1, %2, %0" : "+v"(c) : "v"(a), "v"(b));
}
DEV unsigned pack2(f32x2 v) { return __builtin_bit_cast(unsigned, __builtin_convertvector(v, bf16x2)); }
DEV float blo(unsigned u) { return __uint_as_float(u << 16); }
DEV float bhi(unsigned u) { return __uint_as_float(u & 0xffff0000u); }
DEV f32x2 lo2(f32x4 v) { return __builtin_shufflevector(v, v, 0, 1); }
DEV f32x2 hi2(f32x4 v) { return __builtin_shufflevector(v, v, 2, 3); }
DEV f32x2 fma2(float a, f32x2 b, f32x2 c) { return __builtin_elementwise_fma(f32x2{a, a}, b, c); }
template <int C> DEV float dpp(float x) { return __builtin_bit_cast(float, __builtin_amdgcn_update_dpp(0, __builtin_bit_cast(int, x), C, 0xF, 0xF, true)); }
DEV float max3(float a, float b, float c) { float r; asm("v_max3_f32 %0, %1, %2, %3" : "=v"(r) : "v"(a), "v"(b), "v"(c)); return r; }
template <class F> DEV void rows2(float& a, float& b, F f) {
  auto r = __builtin_amdgcn_permlane16_swap(__float_as_uint(a), __float_as_uint(b), false, false);
  const unsigned x = __float_as_uint(f(__uint_as_float(r[0]), __uint_as_float(r[1])));
  r = __builtin_amdgcn_permlane32_swap(x, x, false, false);
  const unsigned y = __float_as_uint(f(__uint_as_float(r[0]), __uint_as_float(r[1])));
  r = __builtin_amdgcn_permlane16_swap(y, y, false, false);
  a = __uint_as_float(r[0]); b = __uint_as_float(r[1]);
}
DEV int mod7(int v) { return v >= 7 ? v - 7 : v; }
constexpr int slot_of(int s) { return (s + 2) % 3 * kSlot; }

struct Stg {
  unsigned vo[6], dx, dp, dw;
  u32x4 xs, ws;
  unsigned xo, wo;
  int ks, kstep;
  DEV void next() { xo = ks * 128; wo = ks * kstep; ks = ks == 6 ? 0 : ks + 1; }
};
struct P2 {
  unsigned va[4], vpad;
  unsigned aw[3][4];
  unsigned sA0, sA2;
  u32x4 s2, so, sa;
  int rot;
  bf16x8 Wf[5][4];
  unsigned flag;
  unsigned fofs;
  DEV void peek(const char* ws) const { dma_dev(fofs, ws, kScr + 1408 + (sA0 >> 12) * kScrW, 3); }
  const char *w2, *wo;
  int rg, ct;
  bool live;
  DEV void init(int lane, int w, const char* ws) {
    const int r7 = lane >> 3;
    const unsigned v0 = r7 * 896 + (((lane & 7) ^ (r7 & 2)) << 4);
#pragma unroll
    for (int i = 0; i < 4; ++i) { va[i] = (v0 ^ ((i & 1) << 6)) + i * (8 * 896 - 1024); OPAQUE(va[i]); }
    s2 = make_srd(w2 + w * (224 * 896) + ct * 128, live ? 224 * 896 : 0);
    sa = make_srd(ws + kWsA2, 32 * 72 * 4096);
    OPAQUE_S(s2); OPAQUE_S(sa);
  }
  DEV void init2(int lane, int w, unsigned own_flag) {
    const int g = lane >> 4, q = (lane >> 2) & 3, p = lane & 3;
    const unsigned a0 = (8 * g + q) * 128 + (p >> 1) * 16 + (p & 1) * 8 + sA0, sx = 2 * (g & 1) + (q >> 1);
#pragma unroll
    for (int i = 0; i < 4; ++i) {
      aw[0][i] = a0 + (i ^ sx) * 32; aw[1][i] = aw[0][i] + (sA2 - sA0); aw[2][i] = aw[0][i] + 98304;
      OPAQUE(aw[0][i]); OPAQUE(aw[1][i]); OPAQUE(aw[2][i]);
    }
    vpad = lane < 32 ? va[3] : kOut;
    fofs = kWsFlags + (((rg >> 1) * 16 + w) * 2 + (rg & 1)) * 128 + (lane & 1) * 16;
    flag = own_flag;
    OPAQUE(vpad); OPAQUE(fofs); OPAQUE(flag);
    so = make_srd(wo + w * (56 * 896) + ct * 128, live ? 56 * 896 : 0);
    OPAQUE_S(so);
    PIN();
  }
  DEV int kstep(int s) const { return mod7(s + rot); }
  DEV void m0(int s) const { if (s == 2 || s == 6) set_m0(0, sA2); else set_m0(s == 5 ? 98304 : s == 1 || s == 4 || s == 8 ? kSlot : 0, sA0); }
  DEV void go(int s, int i) const {
    if (s < 7) dma(va[i], s2, kstep(s) * (32 * 896), i * 1024);
    else dma(i == 3 ? vpad : va[i], so, (s - 7) * (28 * 896), i * 1024);
  }
  DEV void ld(int s) const {
    m0(s); NOP(0);
#pragma unroll
    for (int i = 0; i < 4; ++i) go(s, i);
  }
  DEV void rd1(int s, int u) {
    const unsigned a = s == 2 || s == 6 ? aw[1][u] : s == 5 ? aw[2][u] : aw[0][u] + (s == 1 || s == 4 || s == 8 ? kSlot : 0);
    Wf[s % 5][u] = lds_tr(a, a + 512);
  }
  DEV void rd(int s) {
#pragma unroll
    for (int u = 0; u < 4; ++u) rd1(s, u);
  }
};

template <bool FFN, int NT>
DEV void phase1(Stg& sg, const char* b1, char* ws, u64 tag64, int lane, int w, int rt, int cg, P2& p2 STAMP_ARG) {
  constexpr int WOFF = FFN ? 16384 : 18432, WROW = FFN ? 448 : 384;
  const int mh = w & 1, m = lane & 15, g = lane >> 4, q = m >> 2, p = lane & 3;
  const int t0 = FFN ? (NT == 4 ? (w >> 1) * 4 : 8 + ((w >> 1) - 2) * 3) : (w >> 1) * 3;
  auto tile = [&](int i) { return t0 + (NT == 4 ? i ^ (2 * mh) : i == 0 ? 2 * mh : i == 1 ? 1 - mh : 2 - mh); };
  const int r0 = rt * 128;
  auto ld_m0 = [&](int l, unsigned slot) { if (l == 0 || l == 2 || l == 3) set_m0(slot, l == 0 ? sg.dx : l == 2 ? sg.dp : sg.dw); };
  auto ld_go = [&](int l) {
    if (l < 2) dma(sg.vo[l], sg.xs, sg.xo, l * 1024);
    else if (l == 2) dma_part(sg.vo[2], FFN ? sg.ws : sg.xs, FFN ? sg.wo : sg.xo, FFN ? 0xffffffffull : 0xffffull);
    else dma(sg.vo[l], sg.ws, sg.wo, (l - 3) * 1024);
  };
  unsigned ax[2], ae[2], aw[NT];
#pragma unroll
  for (int kk = 0; kk < 2; ++kk) { ax[kk] = slot_of(0) + (64 * mh + m) * 128 + (((4 * kk + g) ^ (m >> 1)) << 4); ae[kk] = ax[kk] + (8 - 4 * mh) * 2048; }
#pragma unroll
  for (int i = 0; i < NT; ++i)
    aw[i] = slot_of(0) + WOFF + (8 * g + q) * WROW + (p >> 1) * 16 + (p & 1) * 8 + (tile(i) ^ (FFN ? g & 1 : 2 * (g & 1) + (q >> 1))) * 32;
  const unsigned scr = kScr + w * kScrW;
  unsigned ast, acx[NT], abi[NT], sto, sso[NT];
  unsigned co[3], ce[2], ak0, ak1, av0, av1, oso;
  f32x2 kb[4];
  u32x4 one = {0x3f803f80u, 0x3f803f80u, 0x3f803f80u, 0x3f803f80u};
  OPAQUE(one);
  const char* first;
  auto setup = [&]() {
    p2.init(lane, w, ws);
    ast = scr + m * 32; OPAQUE(ast);
#pragma unroll
    for (int n = 0; n < NT; ++n) { acx[n] = kCex + (tile(n) * 16 + 4 * g) * 4; OPAQUE(acx[n]); }
    if (FFN) {
      sto = (g & 1) * 1024 + ((g >> 1) * 16 + m) * 16; OPAQUE(sto);
#pragma unroll
      for (int n = 0; n < NT; ++n) {
        abi[n] = scr + 1024 + (tile(n) - t0) * 32 + g * 8; OPAQUE(abi[n]);
        sso[n] = ((2 * rt + mh) * 72 + 7 * cg + (tile(n) >> 1)) * 4096 + (tile(n) & 1) * 512; OPAQUE_S(sso[n]);
      }
      const int l0 = ((rt * 8 + cg) * 8 + w) * 16;
      first = (l0 < 12544 ? p2.w2 : p2.wo) + (l0 < 12544 ? l0 : l0 < 15680 ? l0 - 12544 : 0) * 128;
      OPAQUE_S(first);
    }
    PIN();
  };
  auto prep = [&](int i) {
    if (FFN) return;
    const int blk = w >> 1, par = w & 1;
    const unsigned rb = kAtt + (par * 72 + 16 * blk) * 400;
    if (i < 3) {
      const int T = tile(i), n = 16 * (T & 3) + 4 * g;
      const unsigned c = kAtt + (T >> 2) * 128 + (n + (n < 28 ? 0 : n < 56 ? 4 : n < 60 ? -28 : 0)) * 2;
      co[i] = c + (8 + 32 * mh + m) * 400; OPAQUE(co[i]);
      if (i < 2) { ce[i] = c + ((m & 1) * 72 + (m >> 1)) * 400; OPAQUE(ce[i]); }
    } else if (i == 3) { ak0 = rb + m * 400 + 16 * g; ak1 = rb + (16 + (m & 7)) * 400 + 128 + 16 * g; OPAQUE(ak0); OPAQUE(ak1); }
    else if (i == 4) { av0 = rb + (4 * g + q) * 400 + 256 + 16 * p; av1 = rb + (16 + 4 * (g & 1) + q) * 400 + 256 + 16 * p; OPAQUE(av0); OPAQUE(av1); }
    else if (i < 9) {
      const int k = i - 5, d = m + 8 - 4 * g - (16 * (k >> 1) + 2 * (k & 1));
      kb[k] = f32x2{(unsigned)d < 8u ? 0.f : -__builtin_inff(), (unsigned)(d - 1) < 8u ? 0.f : -__builtin_inff()};
      OPAQUE(kb[k]);
    }
  };
  oso = (((2 * rt + (w >> 2)) * 72 + 56 + 2 * cg) * 4 + (w & 3)) * 1024;
  const unsigned tag = (unsigned)tag64;
  f32x4 acc[4][NT], ace[2];
  bf16x8 X[2][4], W[2][NT], XE[2];
  constexpr int NRD = NT + 2 + (FFN ? 0 : 1);
  auto rd1 = [&](int kk, int i) {
    if (i == 0) { X[kk][0] = lds_b128(ax[kk]); X[kk][1] = lds_b128(ax[kk] + 2048); }
    else if (i == 2) { X[kk][2] = lds_b128(ax[kk] + 4096); X[kk][3] = lds_b128(ax[kk] + 6144); }
    else if (i < NT + 2) { const int n = i == 1 ? 0 : i - 2; W[kk][n] = lds_tr(aw[n] + kk * 32 * WROW, aw[n] + (kk * 32 + 4) * WROW); }
    else if (i < NRD) XE[kk] = lds_b128(ae[kk]);
  };
  auto half = [&](int kk, bool first, int nex, bool reads, int adv, auto load_at, unsigned slot, auto hook) {
    u32x4 e = one;
#pragma unroll
    for (int i = 0; i < 4 * NT + 4; ++i) {
      if (i >= 4 * NT + nex) break;
      const int l = load_at(i);
      if (l >= 100) { if ((l & 3) == 0) p2.m0((l - 100) >> 2); } else if (l >= 0) ld_m0(l, slot);
      if (!FFN && i == 4 * NT + 2) { e = m == 0 ? one : __builtin_bit_cast(u32x4, XE[1 - kk]); OPAQUE(e); NOP(1); }
      if (i < 4 * NT) mma(acc[i & 3][i >> 2], W[kk][i >> 2], X[kk][i & 3], first);
      else mma(ace[i & 1], W[i < 4 * NT + 2 ? kk : 1 - kk][i & 1], e, first);
      if (l >= 100) p2.go((l - 100) >> 2, l & 3); else if (l >= 0) ld_go(l);
      if (reads && i % 2 == 0 && i / 2 < NRD) {
        const int r = i / 2;
        if (adv) {
          if (r == 0) { ax[0] += adv; OPAQUE(ax[0]); }
          else if (r == 2) { ax[1] += adv; OPAQUE(ax[1]); }
          else if (r < NT + 2) { const int n = r == 1 ? 0 : r - 2; aw[n] += adv; OPAQUE(aw[n]); }
          else { ae[0] += adv; ae[1] += adv; OPAQUE(ae[0]); OPAQUE(ae[1]); }
        }
        rd1(1 - kk, r);
      }
      if (!FFN && nex && i == 4 * NT - 3) { e = m == 0 ? one : __builtin_bit_cast(u32x4, XE[kk]); OPAQUE(e); }
      hook(i);
      PIN();
    }
  };
  auto ldstats = [&](int i) {
    if (i == 0) dma_dev(lane * 16, ws + kWsStats + (r0 + 64 * mh) * 16, scr, ~0ull);
    else if (FFN) dma_dev(lane * 16, b1 + (cg * 14 + t0) * 32, scr + 1024, (1ull << (2 * NT)) - 1);
    else dma_dev(rt == 0 ? 0 : lane * 16, ws + kWsStats + (rt == 0 ? 0 : r0 - 16) * 16, scr + 1024, 0xffffull);
  };
  u32x4 st[5];
  u32x2 bias[NT];
  f32x4 cs[NT];
  unsigned bad = 0;
  auto rdstats = [&](int i) { st[i] = *(LAS u32x4*)(size_t)(i < 4 ? ast + (i >> 1) * 512 + (i & 1) * 16 : scr + 1024 + m * 16); };
  auto check = [&](int i) { bad |= (st[i][1] ^ tag) | (st[i][3] ^ tag); OPAQUE(bad); };
  constexpr int NST = FFN ? 4 : 5;

  WAIT_VM(8);
  CS(2, 4);
  BARRIER();
  STAMP(1);
  CSTAMP(0);
  CS(2, 5);
#pragma unroll
  for (int i = 0; i < NRD; ++i) rd1(0, i);
  sg.next();
#pragma unroll
  for (int l = 0; l < 3; ++l) { ld_m0(l, slot_of(2)); NOP(0); ld_go(l); }
  PIN();
#pragma unroll
  for (int s = 0; s < 6; ++s) {
    auto three = [](int l0) { return [=](int i) { return i % 4 == 1 && i < 12 ? l0 + i / 4 : -1; }; };
    auto four = [](int l0) { return [=](int i) { return i % 3 == 1 && i < 12 ? l0 + i / 3 : -1; }; };
    auto none = [](int) { return -1; };
    if (s < 5) half(0, s == 0, 2, true, 0, three(3), slot_of(s + 2), [&](int i) { if (s == 3 && (i == 11 || i == 13)) ldstats(i == 13); });
    else half(0, false, 2, true, 0, none, 0, [&](int i) {
      if ((i & 1) && i / 2 < NST) rdstats(i / 2);
      if (i >= 9 && i < 9 + NST) check(i - 9);
    });
    if (s == 0) setup();
    if (FFN && s == 1) touch(lane * 128, first, scr + 1280, 0xffff);
    if (s == 5 && __builtin_expect(__ballot(bad != 0) != 0, 0)) {
#pragma nounroll
      for (int spin = 0; spin < kSpinCap; ++spin) {
        __builtin_amdgcn_s_sleep(2);
        ldstats(0);
        if (!FFN) ldstats(1);
        WAIT_VM(0);
        bad = 0;
#pragma unroll
        for (int i = 0; i < NST; ++i) { rdstats(i); check(i); }
        if (__ballot(bad != 0) == 0) break;
      }
    }
    WAIT_LGKM0();
    if (s < 5) WAIT_VM(6 + (FFN && s == 1) + (s == 3 ? 2 : 0));
    else WAIT_VM(4);
    BARRIER();
    if (s < 6) CS(6, s < 6 ? s : 0);
    if (s >= 1) CS(7, s >= 1 ? s - 1 : 0);
    if (s < 4) sg.next();
    PIN();
    if (s < 4) half(1, false, 2, true, slot_of(s + 1) - slot_of(s), three(0), slot_of(s), [&](int i) { if (s >= 1 && i % 4 == 3) prep(3 * (s - 1) + i / 4); });
    else half(1, false, 2, true, slot_of(s + 1) - slot_of(s), four(100 + 4 * (s - 4)), 0, [&](int) {});
  }
  CSTAMP(2);
  auto blk4 = [&](int kk, int n, auto hook) {
#pragma unroll
    for (int mt = 0; mt < 4; ++mt) { mma(acc[mt][n], W[kk][n], X[kk][mt], false); hook(mt); PIN(); }
  };
  {
    u32x4 e0 = one, e1 = one;
    blk4(0, 0, [&](int i) { rd1(1, i == 0 ? 1 : i == 1 ? 3 : i == 2 ? (FFN ? 0 : NRD - 1) : (FFN ? 2 : 0)); });
    if (!FFN) { e0 = m == 0 ? one : __builtin_bit_cast(u32x4, XE[0]); OPAQUE(e0); }
    blk4(0, 1, [&](int i) {
      if (FFN) { if (i + 4 < NRD) rd1(1, i + 4); bias[i < NT ? i : 0] = *(LAS u32x2*)(size_t)abi[i < NT ? i : 0]; }
      else if (i < 2) rd1(1, i == 0 ? 2 : 4);
    });
    mma(ace[0], W[0][0], e0, false); mma(ace[1], W[0][1], e0, false);
    if (!FFN) { e1 = m == 0 ? one : __builtin_bit_cast(u32x4, XE[1]); OPAQUE(e1); NOP(1); }
    mma(ace[0], W[1][0], e1, false); mma(ace[1], W[1][1], e1, false);
    PIN();
    blk4(0, 2, [&](int) {});
    NOP(3);
    asm volatile("s_mov_b64 exec, %4\n\tds_write_b128 %0, %1\n\tds_write_b128 %2, %3\n\ts_mov_b64 exec, -1\n\ts_waitcnt lgkmcnt(0)"
                 ::"v"(acx[0]), "v"(ace[0]), "v"(acx[1]), "v"(ace[1]), "s"(0x0001000100010001ull) : "memory");
  }
  WAIT_LGKM0();
  BARRIER();
  CS(7, 5);
  CSTAMP(3);
  if (PRIO & 8) asm volatile("s_cmp_lt_u32 %0, 4\n\ts_cbranch_scc1 1f\n\ts_setprio 1\n1:" ::"s"(w) : "scc");
  p2.m0(2);
  blk4(1, 0, [&](int i) { p2.go(2, i); if (i < NT) cs[i] = lds_f4(acx[i]); });
  blk4(1, 1, [&](int) {});
  float rs[5], mu[5];
#pragma unroll
  for (int i = 0; i < NST; ++i) { rs[i] = __uint_as_float(st[i][2]); mu[i] = FFN ? -rs[i] * __uint_as_float(st[i][0]) : -__uint_as_float(st[i][0]); }
  auto epi = [&](int n) {
    if (FFN) {
      const f32x2 b0 = {blo(bias[n][0]), bhi(bias[n][0])}, b1v = {blo(bias[n][1]), bhi(bias[n][1])};
#pragma unroll
      for (int pp = 0; pp < 2; ++pp) {
        unsigned y[2][2];
#pragma unroll
        for (int e = 0; e < 2; ++e) {
          const int mt = 2 * pp + e;
          f32x2 u0 = fma2(rs[mt], lo2(acc[mt][n]), fma2(mu[mt], lo2(cs[n]), b0)), u1 = fma2(rs[mt], hi2(acc[mt][n]), fma2(mu[mt], hi2(cs[n]), b1v));
          u0 = f32x2{fmaxf(u0[0], 0.f), fmaxf(u0[1], 0.f)}; u1 = f32x2{fmaxf(u1[0], 0.f), fmaxf(u1[1], 0.f)};
          y[e][0] = pack2(u0 * u0); y[e][1] = pack2(u1 * u1);
        }
        const auto s0 = __builtin_amdgcn_permlane16_swap(y[0][0], y[1][0], false, false);
        const auto s1 = __builtin_amdgcn_permlane16_swap(y[0][1], y[1][1], false, false);
        store_wt(sto, p2.sa, sso[n], pp * 2048, u32x4{s0[0], s1[0], s0[1], s1[1]});
      }
    } else {
      auto put = [&](const f32x4& a, int i, unsigned addr) {
        *(LAS u32x2*)(size_t)addr = u32x2{pack2(fma2(mu[i], lo2(cs[n]), lo2(a)) * rs[i]), pack2(fma2(mu[i], hi2(cs[n]), hi2(a)) * rs[i])};
      };
      if (n < 2) put(ace[n], 4, ce[n]);
#pragma unroll
      for (int mt = 0; mt < 4; ++mt) put(acc[mt][n], mt, co[n] + ((mt & 1) * 72 + 16 * (mt >> 1)) * 400);
    }
    PIN();
  };
  PIN();
  NOP(3);
  PIN();
  epi(0);
  blk4(1, 2, [&](int) {});
  epi(1);
  if (NT == 4) { blk4(0, 3, [&](int) {}); blk4(1, 3, [&](int) {}); }
  epi(2);
  if (NT == 4) epi(3);
  CSTAMP(4);
  CS(3, 0);
  CS(4, 0);
  STAMP(2);
  p2.init2(lane, w, kWsFlags + ((rt * 16 + cg + (FFN ? 0 : 8)) * 2 + (FFN ? w & 1 : w >> 2)) * 128 + (FFN ? w >> 1 : w & 3) * 8);
  if (FFN) {
    STAMP(3);
    CS(3, 2);
    WAIT_VM(0);
    store_flag(p2.flag, ws, tag64);
    STAMP(4);
    CS(3, 3);
    p2.rd(0);
    p2.rd(1);
    p2.rd(2);
    WAIT_LGKM0();
    BARRIER();
    CS(3, 4);
    p2.ld(3); p2.ld(4); p2.ld(5);
    if (LATE78) { p2.peek(ws); p2.ld(6); }
    else {
      p2.ld(6);
      WAIT_VM(12); p2.rd(3); WAIT_LGKM0();
      p2.ld(7);
      WAIT_VM(12); p2.rd(4);
      p2.peek(ws);
      WAIT_LGKM0();
      p2.ld(8);
    }
    CS(3, 5);
  } else {
    WAIT_VM(4);
    p2.rd(0);
    p2.rd(1);
    WAIT_LGKM0();
    CS(4, 1);
    BARRIER();
    CS(4, 2);
    p2.ld(3);
    const f32x4 z = {0.f, 0.f, 0.f, 0.f};
    f32x4 sc[2][2];
    bf16x8 qf[2], kf[2][2], vf[2][2];
#pragma unroll
    for (int hd = 0; hd < 2; ++hd) { qf[hd] = lds_b128(ak0 + 8 * 400 + hd * 64); kf[hd][0] = lds_b128(ak0 + 128 + hd * 64); kf[hd][1] = lds_b128(ak1 + hd * 64); }
#pragma unroll
    for (int hd = 0; hd < 2; ++hd) { vf[hd][0] = lds_tr(av0 + hd * 64, av1 + hd * 64); vf[hd][1] = lds_tr(av0 + hd * 64 + 8, av1 + hd * 64 + 8); }
    PIN();
#pragma unroll
    for (int hd = 0; hd < 2; ++hd) { sc[hd][0] = mfma(kf[hd][0], qf[hd], z); sc[hd][1] = mfma(kf[hd][1], qf[hd], z); }
    PIN();
    p2.ld(4);
    WAIT_VM(8);
    p2.rd(2);
    WAIT_LGKM0();
    BARRIER();
    CS(4, 3);
    p2.ld(5);
    PIN();
    f32x2 e[2][4];
    float mx[2], sum[2];
    const float kLog = 0.18898223650461362f * 1.4426950408889634f;
#pragma unroll
    for (int hd = 0; hd < 2; ++hd) {
#pragma unroll
      for (int t = 0; t < 2; ++t) {
        e[hd][2 * t] = fma2(kLog, lo2(sc[hd][t]), kb[2 * t]);
        e[hd][2 * t + 1] = fma2(kLog, hi2(sc[hd][t]), kb[2 * t + 1]);
      }
    }
    if (__builtin_expect(r0 + 32 * (w >> 1) == 0, 0)) {
#pragma unroll
      for (int i = 0; i < 2; ++i) {
        const f32x2 v = kb[i] + -1.0e9f * 1.4426950408889634f;
        if (g < 2) e[0][i] = e[1][i] = v;
      }
    }
#pragma unroll
    for (int hd = 0; hd < 2; ++hd) {
      mx[hd] = max3(max3(e[hd][0][0], e[hd][0][1], e[hd][1][0]), max3(e[hd][1][1], e[hd][2][0], e[hd][2][1]), max3(e[hd][3][0], e[hd][3][1], e[hd][3][1]));
    }
    rows2(mx[0], mx[1], [](float a, float b) { return max3(a, b, b); });
    PIN();
    if (!LATE78) p2.ld(6);
    PIN();
#pragma unroll
    for (int hd = 0; hd < 2; ++hd) {
#pragma unroll
      for (int i = 0; i < 4; ++i) { const f32x2 d = e[hd][i] - mx[hd]; e[hd][i] = f32x2{__builtin_amdgcn_exp2f(d[0]), __builtin_amdgcn_exp2f(d[1])}; }
      const f32x2 t = (e[hd][0] + e[hd][1]) + (e[hd][2] + e[hd][3]);
      sum[hd] = t[0] + t[1];
    }
    rows2(sum[0], sum[1], [](float a, float b) { return a + b; });
    f32x4 o[2][2];
#pragma unroll
    for (int hd = 0; hd < 2; ++hd) {
      const float inv = __builtin_amdgcn_rcpf(sum[hd]);
      const u32x4 pf = {pack2(e[hd][0] * inv), pack2(e[hd][1] * inv), pack2(e[hd][2] * inv), pack2(e[hd][3] * inv)};
      o[hd][0] = mfma(vf[hd][0], __builtin_bit_cast(bf16x8, pf), z);
      o[hd][1] = mfma(vf[hd][1], __builtin_bit_cast(bf16x8, pf), z);
    }
    if (!LATE78) { WAIT_VM(8); p2.rd(3); p2.rd(4); }
#pragma unroll
    for (int hd = 0; hd < 2; ++hd)
      store_wt(lane * 16, p2.sa, oso + hd * 4096, 0, u32x4{pack2(lo2(o[hd][0])), pack2(hi2(o[hd][0])), pack2(lo2(o[hd][1])), pack2(hi2(o[hd][1]))});
    STAMP(3);
    CS(4, 4);
    if (LATE78) { p2.peek(ws); p2.ld(6); WAIT_VM(5); store_flag(p2.flag, ws, tag64); }
    else {
      WAIT_LGKM0();
      p2.ld(7);
      WAIT_VM(4);
      store_flag(p2.flag, ws, tag64);
      p2.peek(ws);
      p2.ld(8);
    }
    STAMP(4);
    CS(4, 5);
  }
}

__global__ __launch_bounds__(512, 2) void fused_kernel(const char*, const char*, const char*, const char*, const char*, const char*, const char*, char*, char*) {
  STAMP_INIT();
  STAMP(0);
  u32x16 ka;
  u64 kb;
  asm volatile("s_load_dwordx16 %0, %2, 0x0\n\ts_load_dwordx2 %1, %2, 0x40" : "=&s"(ka), "=&s"(kb) : "s"(__builtin_amdgcn_kernarg_segment_ptr()));
  __shared__ __attribute__((aligned(16))) char smem[kLds];
  asm volatile("" ::"v"((LAS char*)smem));
  const int lane = threadIdx.x & 63, w = __builtin_amdgcn_readfirstlane(threadIdx.x >> 6);
  const int bid = blockIdx.x, xcd = bid & 7, j = bid >> 3;
  if (PRIO & 1) asm volatile("s_cmp_lt_u32 %0, 4\n\ts_cbranch_scc1 1f\n\ts_setprio 1\n1:" ::"s"(w) : "scc");
  const int rt = (xcd & 1) * 8 + (j & 7), cgi = j >> 3, cg = 2 * (xcd >> 1) + (cgi & 1);
  const bool ffn = cgi < 2;
  const char *x, *wo, *b1, *w2, *b2;
  char *out, *ws;
  Stg sg;
  u64 tag64;
  {
    const int r0 = rt * 128;
#pragma unroll
    for (int u = 0; u < 2; ++u) {
      const unsigned r = 8 * u + (lane >> 3);
      sg.vo[u] = __umul24(r0 + 32 * (w >> 1) + 2 * r + (w & 1), 896) + (((lane & 7) ^ (r >> 1)) << 4) - u * 1024;
    }
    sg.dx = w * 2048;
    sg.ks = mod7((rt & 3) * 2 + (xcd >> 1));
    asm volatile("" : "+v"(sg.vo[0]), "+v"(sg.vo[1]), "+s"(sg.ks), "+s"(sg.dx));
    asm volatile("s_waitcnt lgkmcnt(0)" : "+s"(ka), "+s"(kb));
    auto ptr = [&](int i) { return (char*)(((u64)ka[2 * i + 1] << 32) | ka[2 * i]); };
    x = ptr(0); wo = ptr(2); b1 = ptr(4); w2 = ptr(5); b2 = ptr(6); out = ptr(7); ws = (char*)kb;
    CS(2, 0);
    sg.xs = make_srd(x, 2048 * 896);
    set_m0(slot_of(0), sg.dx);
    NOP(0);
    dma(sg.vo[0], sg.xs, sg.ks * 128, 0);
    dma(sg.vo[1], sg.xs, sg.ks * 128, 1024);
    PIN();
    const unsigned q0 = 48 * w + (lane >> 2), l3 = (lane & 3) << 4;
    if (ffn) {
      auto woff = [&](unsigned Q) { const unsigned k = __umul24(Q, 9363) >> 16; return __umul24(k, 3584 - 448) + Q * 64 + cg * 448 + (l3 ^ ((k << 2) & 32)); };
#pragma unroll
      for (int u = 0; u < 3; ++u) sg.vo[3 + u] = woff(q0 + 16 * u) - u * 1024;
      sg.vo[2] = woff(384 + 8 * w + ((lane >> 2) & 7));
    } else {
#pragma unroll
      for (int u = 0; u < 3; ++u) {
        const unsigned Q = q0 + 16 * u, k = __umul24(Q, 10923) >> 16, cq = Q - 6 * k, c = (((cq & 1) << 6) | l3) ^ ((k & 2) << 4) ^ ((k & 8) << 3);
        sg.vo[3 + u] = (c == 112 ? kOut : __umul24(k, 2688) + __umul24(cq >> 1, 896) + cg * 112 + c) - u * 1024;
      }
      const int hr = r0 - 16 + 2 * w + ((lane >> 3) & 1);
      sg.vo[2] = __umul24(hr < 0 ? 0 : hr, 896) + (((lane & 7) ^ w) << 4);
    }
    sg.dw = (ffn ? 16384 : 18432) + w * 3072;
    sg.dp = ffn ? 16384 + 24576 + w * 512 : 16384 + w * 256;
    sg.kstep = ffn ? 64 * 3584 : 64 * 2688;
    sg.ws = make_srd(ffn ? ptr(3) : ptr(1), 7 * sg.kstep);
    const u32x4 ps = ffn ? sg.ws : sg.xs;
    const u64 part = ffn ? 0xffffffffull : 0xffffull;
    auto ld = [&](int s, int l) {
      if (l == 0 || l == 2 || l == 3) set_m0(slot_of(s), l == 0 ? sg.dx : l == 2 ? sg.dp : sg.dw);
      if (l == 0 || l == 3) NOP(0);
      if (l < 2) dma(sg.vo[l], sg.xs, sg.xo, l * 1024);
      else if (l == 2) dma_part(sg.vo[2], ps, ffn ? sg.wo : sg.xo, part);
      else dma(sg.vo[l], sg.ws, sg.wo, (l - 3) * 1024);
    };
    const int strow = (xcd & 1) * 1024 + (xcd >> 1) * 256 + j * 8 + w;
    u32x2 ep;
    u32x4 srow;
    asm volatile("s_mov_b64 exec, 1\n\tglobal_load_dwordx2 %0, %1, %2 sc1\n\ts_mov_b64 exec, -1" : "=v"(ep) : "v"(0), "s"(ws) : "memory");
    asm volatile("buffer_load_dwordx4 %0, %1, %2, %3 offen" : "=v"(srow) : "v"(lane < 56 ? lane * 16 : kOut), "s"(sg.xs), "s"(strow * 896) : "memory");
    sg.next();
    ld(0, 3); ld(0, 4); ld(0, 5); ld(0, 2);
    {
      const char* pc;
      unsigned nl;
      asm volatile("s_getpc_b64 %0\n.Lfk_pc:\n\ts_mov_b32 %1, (.Lfk_end - .Lfk_pc) / 128" : "=s"(pc), "=s"(nl));
      const unsigned li = (j * 8 + w) * 2;
      touch((li + lane) * 128, pc, kScr + w * kScrW + 1280, (1u << (min(nl, li + 2) - min(nl, li))) - 1);
    }
    CS(2, 1);
    asm volatile("s_waitcnt vmcnt(5)" : "+v"(srow), "+v"(ep)::"memory");
    tag64 = (((u64)__builtin_amdgcn_readfirstlane(ep[1]) << 32) | (unsigned)__builtin_amdgcn_readfirstlane(ep[0])) + 1;
    float sm = 0.f, sq = 0.f;
#pragma unroll
    for (int i = 0; i < 4; ++i) {
      const unsigned u = srow[i];
      const bf16x2 v = __builtin_bit_cast(bf16x2, u);
      sm = __builtin_amdgcn_fdot2_f32_bf16(v, __builtin_bit_cast(bf16x2, 0x3f803f80u), sm, false);
      sq = __builtin_amdgcn_fdot2_f32_bf16(v, v, sq, false);
    }
    sm += dpp<0xB1>(sm); sq += dpp<0xB1>(sq);
    sm += dpp<0x4E>(sm); sq += dpp<0x4E>(sq);
    sm += dpp<0x141>(sm); sq += dpp<0x141>(sq);
    sm += dpp<0x140>(sm); sq += dpp<0x140>(sq);
    rows2(sm, sq, [](float a, float b) { return a + b; });
    sm *= 1.f / 448.f;
    store_wt_lane0(kWsStats + strow * 16, ws,
                   u32x4{__float_as_uint(sm), (unsigned)tag64, __float_as_uint(__builtin_amdgcn_rsqf(fmaxf(fmaf(-sm, sm, sq * (1.f / 448.f)), 0.f) + 1e-5f)), (unsigned)tag64});
    CS(2, 3);
    sg.next();
#pragma unroll
    for (int l = 0; l < 6; ++l) ld(1, l);
    CS(2, 2);
  }
  const int rg = 4 * xcd + (j & 3), ct = mod7(mod7(7 + (j < 28 ? j >> 2 : 6) - 2 * (j & 3)));
  const int m = lane & 15, g = lane >> 4;
  P2 p2;
  p2.sA0 = w * 4096; p2.sA2 = (w < 3 ? 32768 : 65536) + w * 4096; p2.rot = 2 * (rg & 3);
  p2.w2 = w2; p2.wo = wo; p2.rg = rg; p2.ct = ct; p2.live = j < 28;

  if (!ffn) phase1<false, 3>(sg, b1, ws, tag64, lane, w, rt, cg, p2 STAMP_PASS);
  else if (w < 4) phase1<true, 4>(sg, b1, ws, tag64, lane, w, rt, cg, p2 STAMP_PASS);
  else phase1<true, 3>(sg, b1, ws, tag64, lane, w, rt, cg, p2 STAMP_PASS);

  if (j >= 28) {
    if (bid == 255) {
      const unsigned t0 = (unsigned)tag64, t1 = (unsigned)(tag64 >> 32);
#pragma nounroll
      for (int spin = 0; spin < kSpinCap; ++spin) {
        u32x4 f, h;
        asm volatile("global_load_dwordx4 %0, %2, %3 sc1\n\tglobal_load_dwordx4 %1, %2, %4 sc1\n\ts_waitcnt vmcnt(0)" : "=&v"(f), "=&v"(h)
                     : "v"((lane >> 1) * 128 + (lane & 1) * 16), "s"(ws + kWsFlags + w * 8192), "s"(ws + kWsFlags + w * 8192 + 4096) : "memory");
        if (__all(f[0] == t0 && f[1] == t1 && f[2] == t0 && f[3] == t1 && h[0] == t0 && h[1] == t1 && h[2] == t0 && h[3] == t1)) break;
        __builtin_amdgcn_s_sleep(8);
      }
      BARRIER();
      if (w == 0) store_flag(0, ws, tag64);
    }
    STAMP(7);
    STAMP_OUT();
    return;
  }

  const unsigned fofs = kWsFlags + (((rg >> 1) * 16 + w) * 2 + (rg & 1)) * 128 + (lane & 3) * 8;
  auto ready = [&](u32x2 f) { return (__ballot(f[0] == (unsigned)tag64 && f[1] == (unsigned)(tag64 >> 32)) & 15) == 15; };
  auto poll = [&](unsigned ofs) {
#pragma nounroll
    for (int spin = 0; spin < kSpinCap; ++spin) {
      u32x2 f;
      asm volatile("s_mov_b64 exec, 15\n\tglobal_load_dwordx2 %0, %1, %2 sc1\n\ts_mov_b64 exec, -1\n\ts_waitcnt vmcnt(0)" : "=&v"(f) : "v"(ofs), "s"(ws) : "memory");
      if (ready(f)) break;
      __builtin_amdgcn_s_sleep(1);
    }
  };
  CS(5, 0);
  WAIT_VM(4);
  if (!ready(*(LAS u32x2*)(size_t)(kScr + 1408 + w * kScrW + (lane & 3) * 8))) poll(fofs);
  STAMP(5);
  u32x4 H[WIN][4];
  auto hso = [&](int i) { return (unsigned)(rg * 72 + (i < 7 ? 7 * w + p2.kstep(i) : 56 + 2 * w + (i - 7))) * 4096; };
  auto ldh = [&](int i, int mt, unsigned so) {
    asm volatile("buffer_load_dwordx4 %0, %1, %2, %3 offen offset:%4 sc1" : "=v"(H[i % WIN][mt]) : "v"(lane * 16), "s"(p2.sa), "s"(so), "i"(mt * 1024) : "memory");
  };
  CS(5, 1);
  if (PRIO & 4) asm volatile("s_cmp_lt_u32 %0, 4\n\ts_cbranch_scc1 1f\n\ts_setprio 1\n1:" ::"s"(w) : "scc");
  CS(10, 0);
#pragma unroll
  for (int i = 0; i < WIN; ++i) {
    const unsigned so = hso(i);
#pragma unroll
    for (int mt = 0; mt < 4; ++mt) ldh(i, mt, so);
  }
  const int os = threadIdx.x >> 3, orow = rg * 64 + 32 * (os >> 5) + 2 * (os & 15) + ((os >> 4) & 1);
  unsigned oofs = __umul24(orow, 896) + ct * 128 + (threadIdx.x & 7) * 16;
  u32x4 xr, br;
  unsigned pa[3][4], ra[2][2];
#pragma unroll
  for (int u = 0; u < 4; ++u) {
    const unsigned a = m * 256 + (((4 * u + g) ^ (m & 7)) << 4);
    pa[0][u] = a + p2.sA0; pa[1][u] = a + p2.sA0 + 98304; pa[2][u] = a + p2.sA2;
    OPAQUE(pa[0][u]); OPAQUE(pa[1][u]); OPAQUE(pa[2][u]);
  }
  ra[0][0] = (w < 2 ? 0 : w < 4 ? kSlot : w < 6 ? 98304 : 32768) + (os & 15) * 256 + (((2 * (threadIdx.x & 7)) ^ (os & 7)) << 4);
  ra[0][1] = ra[0][0] ^ 16;
  ra[1][0] = ra[0][0] + (w < 6 ? 0 : 32768);
  ra[1][1] = ra[1][0] ^ 16;
  OPAQUE(ra[0][0]); OPAQUE(ra[0][1]); OPAQUE(ra[1][0]); OPAQUE(ra[1][1]); OPAQUE(oofs);
  f32x4 acc[4][4], lo, hi;
  u32x2 fq;
  auto park = [&](int mt, int u) { *(LAS f32x4*)(size_t)(mt < 2 ? pa[0][u] + mt * kSlot : pa[mt - 1][u]) = acc[mt][u]; };
#pragma unroll
  for (int i = 0; i < 9; ++i) {
    const unsigned so = i + WIN < 9 ? hso(i + WIN) : 0;
    if (i == 7 - WIN) {
      asm volatile("s_waitcnt vmcnt(6)" : "+v"(fq)::"memory");
      if (!ready(fq)) poll(fofs + 2048);
    }
#pragma unroll
    for (int mt = 0; mt < 4; ++mt) {
      const int left = 4 * (8 - i) + 3 - mt;
      const int younger = (left < 4 * WIN - 1 ? left : 4 * WIN - 1) + (LATE78 && i >= 1 && i <= WIN ? 8 : 0);
      asm volatile("s_waitcnt vmcnt(%1)" : "+v"(H[i % WIN][mt]) : "i"(younger) : "memory");
      if (mt == 0 && i % 2 == 0) CS(10, 1 + i / 2);
#pragma unroll
      for (int u = 0; u < 4; ++u) {
        mma(acc[mt][u], p2.Wf[i % 5][u], H[i % WIN][mt], i == 0);
        if (LATE78 && i == 0 && u < 2) p2.rd1(3 + u, mt);
        if (u == 0 && i >= 1 && i < 3) p2.rd1(i + 4, mt);
        if (u == 0 && i >= 4 && i < 6) p2.rd1(i + 3, mt);
        if (i == 7 && mt == 0 && u == 0) {
          OPAQUE(xr); OPAQUE(br);
          lo = f32x4{blo(xr[0]) + blo(br[0]), bhi(xr[0]) + bhi(br[0]), blo(xr[1]) + blo(br[1]), bhi(xr[1]) + bhi(br[1])};
          hi = f32x4{blo(xr[2]) + blo(br[2]), bhi(xr[2]) + bhi(br[2]), blo(xr[3]) + blo(br[3]), bhi(xr[3]) + bhi(br[3])};
          OPAQUE(lo); OPAQUE(hi);
        }
        if (i == 8 && mt >= 1) park(mt - 1, u);
        PIN();
      }
      if (i + WIN < 9) ldh(i, mt, so);
    }
    if (LATE78 && i == 0) { WAIT_LGKM0(); p2.ld(7); p2.ld(8); }
    if (i == 5 - WIN) asm volatile("s_mov_b64 exec, 15\n\tglobal_load_dwordx2 %0, %1, %2 offset:2048 sc1\n\ts_mov_b64 exec, -1" : "=v"(fq) : "v"(fofs), "s"(ws) : "memory");
    if (i == 6 - WIN) {
      asm volatile("global_load_dwordx4 %0, %1, %2" : "=v"(xr) : "v"(oofs), "s"(x) : "memory");
      asm volatile("global_load_dwordx4 %0, %1, %2" : "=v"(br) : "v"(ct * 128 + (threadIdx.x & 7) * 16), "s"(b2) : "memory");
    }
  }
  CS(5, 2);
  STAMP(6);
  NOP(6);
  PIN();
#pragma unroll
  for (int u = 0; u < 4; ++u) park(3, u);
  WAIT_LGKM0();
  CS(5, 3);
  BARRIER();
  CS(5, 4);
#pragma unroll
  for (int k = 0; k < 8; k += 2) {
    lo += lds_f4(ra[k >= 3][0] + k * 4096) + lds_f4(ra[k + 1 >= 3][0] + (k + 1) * 4096);
    hi += lds_f4(ra[k >= 3][1] + k * 4096) + lds_f4(ra[k + 1 >= 3][1] + (k + 1) * 4096);
  }
  *(__attribute__((address_space(1))) u32x4*)(out + oofs) = u32x4{pack2(lo2(lo)), pack2(hi2(lo)), pack2(lo2(hi)), pack2(hi2(hi))};
  CS(5, 5);
  STAMP(7);
  STAMP_OUT();
  asm volatile(".Lfk_end:");
}

extern "C" void kernel_launch(void* const* d_in, const int* in_sizes, int n_in, void* d_out, int out_size, void* d_ws, size_t ws_size, hipStream_t stream) {
  fused_kernel<<<256, 512, 0, stream>>>((const char*)d_in[0], (const char*)d_in[1], (const char*)d_in[2], (const char*)d_in[4], (const char*)d_in[5],
                                        (const char*)d_in[6], (const char*)d_in[7], (char*)d_out, (char*)d_ws);
}
```

```cpp
#include <hip/hip_runtime.h>
#include <hip/hip_bf16.h>


typedef __bf16 bf16x2 __attribute__((ext_vector_type(2)));
typedef __bf16 bf16x4 __attribute__((ext_vector_type(4)));
typedef __bf16 bf16x8 __attribute__((ext_vector_type(8)));
typedef float f32x2 __attribute__((ext_vector_type(2)));
typedef float f32x4 __attribute__((ext_vector_type(4)));
typedef unsigned u32x2 __attribute__((ext_vector_type(2)));
typedef unsigned u32x4 __attribute__((ext_vector_type(4)));
typedef unsigned u32x16 __attribute__((ext_vector_type(16)));
typedef unsigned long long u64;
#define LAS __attribute__((address_space(3)))
#define DEV __device__ __forceinline__

constexpr int kLds = 163840;
constexpr int kSlot = 45056;
constexpr int kCex = kLds - 1024;
constexpr int kAtt = kCex - 144 * 400;
constexpr int kScr = 3 * kSlot, kScrW = 1536;
constexpr int kWsFlags = 4096, kWsStats = 131072, kWsA2 = 262144;
constexpr int kSpinCap = 1 << 16;
constexpr int WIN = 2;
constexpr unsigned kOut = 0x80000000u;

#define PIN() __builtin_amdgcn_sched_barrier(0)
#define OPAQUE(x) asm volatile("" : "+v"(x))
#define OPAQUE_S(x) asm volatile("" : "+s"(x))
#define NOP(n) asm volatile("s_nop %0" ::"i"(n))
#define WAIT_VM(n) asm volatile("s_waitcnt vmcnt(%0)" ::"i"(n) : "memory")
#define WAIT_LGKM0() do { asm volatile("" ::: "memory"); __builtin_amdgcn_s_waitcnt(0xC07F); asm volatile("" ::: "memory"); } while (0)
#define BARRIER() do { asm volatile("" ::: "memory"); __builtin_amdgcn_s_barrier(); asm volatile("" ::: "memory"); } while (0)

DEV u32x4 make_srd(const char* p, unsigned bytes) { return u32x4{(unsigned)(u64)p, (unsigned)((u64)p >> 32) & 0xffffu, bytes, 0x00020000u}; }
DEV void set_m0(unsigned a, unsigned b) {
  if (__builtin_constant_p(a)) asm volatile("s_add_u32 m0, %1, %0" ::"i"(a), "s"(b) : "scc");
  else asm volatile("s_add_u32 m0, %0, %1" ::"s"(a), "s"(b) : "scc");
}
DEV void dma(unsigned voff, u32x4 srd, unsigned soff, int ofs) {
  asm volatile("buffer_load_dwordx4 %0, %1, %2 offen offset:%3 lds" ::"v"(voff), "s"(srd), "s"(soff), "i"(ofs) : "memory");
}
DEV void dma_part(unsigned voff, u32x4 srd, unsigned soff, u64 lanes) {
  asm volatile("s_mov_b64 exec, %3\n\tbuffer_load_dwordx4 %0, %1, %2 offen lds\n\ts_mov_b64 exec, -1" ::"v"(voff), "s"(srd), "s"(soff), "s"(lanes) : "memory");
}
DEV void dma_dev(unsigned voff, const char* sbase, unsigned dst, u64 lanes) {
  asm volatile("s_mov_b32 m0, %2\n\ts_mov_b64 exec, %3\n\tglobal_load_lds_dwordx4 %0, %1 sc1\n\ts_mov_b64 exec, -1" ::"v"(voff), "s"(sbase), "s"(dst), "s"(lanes) : "memory");
}
DEV void touch(unsigned voff, const char* sbase, unsigned dst, u64 lanes) {
  asm volatile("s_mov_b32 m0, %2\n\ts_mov_b64 exec, %3\n\tglobal_load_lds_dword %0, %1\n\ts_mov_b64 exec, -1" ::"v"(voff), "s"(sbase), "s"(dst), "s"(lanes) : "memory");
}
DEV void store_wt(unsigned voff, u32x4 srd, unsigned soff, int ofs, u32x4 v) {
  asm volatile("buffer_store_dwordx4 %0, %1, %2, %3 offen offset:%4 sc1\n\ts_nop 1" ::"v"(v), "v"(voff), "s"(srd), "s"(soff), "i"(ofs) : "memory");
}
DEV void store_wt_lane0(unsigned voff, const char* sbase, u32x4 v) {
  asm volatile("s_mov_b64 exec, 1\n\tglobal_store_dwordx4 %0, %1, %2 sc1\n\ts_mov_b64 exec, -1\n\ts_nop 0" ::"v"(voff), "v"(v), "s"(sbase) : "memory");
}
DEV void store_flag(unsigned voff, const char* sbase, u64 v) {
  asm volatile("s_mov_b64 exec, 1\n\tglobal_store_dwordx2 %0, %1, %2 sc1\n\ts_mov_b64 exec, -1" ::"v"(voff), "v"(v), "s"(sbase) : "memory");
}

DEV bf16x8 lds_b128(unsigned a) { return *(LAS bf16x8*)(size_t)a; }
DEV f32x4 lds_f4(unsigned a) { return *(LAS f32x4*)(size_t)a; }
DEV bf16x8 lds_tr(unsigned a0, unsigned a1) {
  bf16x4 a = __builtin_amdgcn_ds_read_tr16_b64_v4bf16((LAS bf16x4*)(size_t)a0);
  bf16x4 b = __builtin_amdgcn_ds_read_tr16_b64_v4bf16((LAS bf16x4*)(size_t)a1);
  return __builtin_shufflevector(a, b, 0, 1, 2, 3, 4, 5, 6, 7);
}
DEV f32x4 mfma(bf16x8 a, bf16x8 b, f32x4 c) { return __builtin_amdgcn_mfma_f32_16x16x32_bf16(a, b, c, 0, 0, 0); }
template <class A, class B> DEV void mma(f32x4& c, A a, B b, bool first) {
  if (first) asm volatile("v_mfma_f32_16x16x32_bf16 %0, %1, %2, 0" : "=&v"(c) : "v"(a), "v"(b));
  else asm volatile("v_mfma_f32_16x16x32_bf16 %0, %1, %2, %0" : "+v"(c) : "v"(a), "v"(b));
}
DEV unsigned pack2(f32x2 v) { return __builtin_bit_cast(unsigned, __builtin_convertvector(v, bf16x2)); }
DEV float blo(unsigned u) { return __uint_as_float(u << 16); }
DEV float bhi(unsigned u) { return __uint_as_float(u & 0xffff0000u); }
DEV f32x2 lo2(f32x4 v) { return __builtin_shufflevector(v, v, 0, 1); }
DEV f32x2 hi2(f32x4 v) { return __builtin_shufflevector(v, v, 2, 3); }
DEV f32x2 fma2(float a, f32x2 b, f32x2 c) { return __builtin_elementwise_fma(f32x2{a, a}, b, c); }
template <int C> DEV float dpp(float x) { return __builtin_bit_cast(float, __builtin_amdgcn_update_dpp(0, __builtin_bit_cast(int, x), C, 0xF, 0xF, true)); }
DEV float max3(float a, float b, float c) { float r; asm("v_max3_f32 %0, %1, %2, %3" : "=v"(r) : "v"(a), "v"(b), "v"(c)); return r; }
DEV float max3_pad(float a, float b, float c) { float r; asm("v_max3_f32 %0, %1, %2, %3\n\ts_nop 1" : "=v"(r) : "v"(a), "v"(b), "v"(c)); return r; }
template <class F> DEV void rows2(float& a, float& b, F f) {
  auto r = __builtin_amdgcn_permlane16_swap(__float_as_uint(a), __float_as_uint(b), false, false);
  const unsigned x = __float_as_uint(f(__uint_as_float(r[0]), __uint_as_float(r[1])));
  r = __builtin_amdgcn_permlane32_swap(x, x, false, false);
  const unsigned y = __float_as_uint(f(__uint_as_float(r[0]), __uint_as_float(r[1])));
  r = __builtin_amdgcn_permlane16_swap(y, y, false, false);
  a = __uint_as_float(r[0]); b = __uint_as_float(r[1]);
}
template <int I> struct Int { static constexpr int v = I; };
template <int I, int N, class F> DEV void static_for(F f) { if constexpr (I < N) { f(Int<I>{}); static_for<I + 1, N>(f); } }
DEV int mod7(int v) { return v >= 7 ? v - 7 : v; }
constexpr int slot_of(int s) { return (s + 2) % 3 * kSlot; }

struct Stg {
  unsigned vo[6], dx, dp, dw;
  u32x4 xs, ws;
  unsigned xo, wo;
  int ks, kstep;
  DEV void next() { xo = ks * 128; wo = ks * kstep; ks = ks == 6 ? 0 : ks + 1; }
};
struct P2 {
  unsigned va[4], vpad;
  unsigned aw[3][4];
  unsigned sA0, sA2;
  u32x4 s2, so, sa;
  int rot;
  bf16x8 Wf[5][4];
  unsigned flag;
  unsigned fofs;
  DEV void peek(const char* ws) const { dma_dev(fofs, ws, kScr + 1408 + (sA0 >> 12) * kScrW, 3); }
  const char *w2, *wo;
  int rg, ct;
  bool live;
  DEV void init(int lane, int w, const char* ws) {
    const int r7 = lane >> 3;
    const unsigned v0 = r7 * 896 + (((lane & 7) ^ (r7 & 2)) << 4);
#pragma unroll
    for (int i = 0; i < 4; ++i) { va[i] = (v0 ^ ((i & 1) << 6)) + i * (8 * 896 - 1024); OPAQUE(va[i]); }
    s2 = make_srd(w2 + w * (224 * 896) + ct * 128, live ? 224 * 896 : 0);
    sa = make_srd(ws + kWsA2, 32 * 72 * 4096);
    OPAQUE_S(s2); OPAQUE_S(sa);
  }
  DEV void init2(int lane, int w, unsigned own_flag) {
    const int g = lane >> 4, q = (lane >> 2) & 3, p = lane & 3;
    const unsigned a0 = (8 * g + q) * 128 + (p >> 1) * 16 + (p & 1) * 8 + sA0, sx = 2 * (g & 1) + (q >> 1);
#pragma unroll
    for (int i = 0; i < 4; ++i) {
      aw[0][i] = a0 + (i ^ sx) * 32; aw[1][i] = aw[0][i] + (sA2 - sA0); aw[2][i] = aw[0][i] + 98304;
      OPAQUE(aw[0][i]); OPAQUE(aw[1][i]); OPAQUE(aw[2][i]);
    }
    vpad = lane < 32 ? va[3] : kOut;
    fofs = kWsFlags + (((rg >> 1) * 16 + w) * 2 + (rg & 1)) * 128 + (lane & 1) * 16;
    flag = own_flag;
    OPAQUE(vpad); OPAQUE(fofs); OPAQUE(flag);
    so = make_srd(wo + w * (56 * 896) + ct * 128, live ? 56 * 896 : 0);
    OPAQUE_S(so);
    PIN();
  }
  DEV int kstep(int s) const { return mod7(s + rot); }
  DEV void m0(int s) const { if (s == 2 || s == 6) set_m0(0, sA2); else set_m0(s == 5 ? 98304 : s == 1 || s == 4 || s == 8 ? kSlot : 0, sA0); }
  DEV void go(int s, int i) const {
    if (s < 7) dma(va[i], s2, kstep(s) * (32 * 896), i * 1024);
    else dma(i == 3 ? vpad : va[i], so, (s - 7) * (28 * 896), i * 1024);
  }
  DEV void ld(int s) const {
    m0(s); NOP(0);
#pragma unroll
    for (int i = 0; i < 4; ++i) go(s, i);
  }
  DEV void rd1(int s, int u) {
    const unsigned a = s == 2 || s == 6 ? aw[1][u] : s == 5 ? aw[2][u] : aw[0][u] + (s == 1 || s == 4 || s == 8 ? kSlot : 0);
    Wf[s % 5][u] = lds_tr(a, a + 512);
  }
  DEV void rd(int s) {
#pragma unroll
    for (int u = 0; u < 4; ++u) rd1(s, u);
  }
};

template <bool FFN, int NT>
DEV void phase1(Stg& sg, const char* b1, char* ws, u64 tag64, int lane, int w, int rt, int cg, P2& p2) {
  constexpr int WOFF = FFN ? 16384 : 18432, WROW = FFN ? 448 : 384;
  const int mh = w & 1, m = lane & 15, g = lane >> 4, q = m >> 2, p = lane & 3;
  const int t0 = FFN ? (NT == 4 ? (w >> 1) * 4 : 8 + ((w >> 1) - 2) * 3) : (w >> 1) * 3;
  auto tile = [&](int i) { return t0 + (NT == 4 ? i ^ (2 * mh) : i == 0 ? 2 * mh : i == 1 ? 1 - mh : 2 - mh); };
  const int r0 = rt * 128;
  auto ld_m0 = [&](int l, unsigned slot) { if (l == 0 || l == 2 || l == 3) set_m0(slot, l == 0 ? sg.dx : l == 2 ? sg.dp : sg.dw); };
  auto ld_go = [&](int l) {
    if (l < 2) dma(sg.vo[l], sg.xs, sg.xo, l * 1024);
    else if (l == 2) dma_part(sg.vo[2], FFN ? sg.ws : sg.xs, FFN ? sg.wo : sg.xo, FFN ? 0xffffffffull : 0xffffull);
    else dma(sg.vo[l], sg.ws, sg.wo, (l - 3) * 1024);
  };
  unsigned ax[2], ae[2], aw[NT];
#pragma unroll
  for (int kk = 0; kk < 2; ++kk) { ax[kk] = slot_of(0) + (64 * mh + m) * 128 + (((4 * kk + g) ^ (m >> 1)) << 4); ae[kk] = ax[kk] + (8 - 4 * mh) * 2048; }
#pragma unroll
  for (int i = 0; i < NT; ++i)
    aw[i] = slot_of(0) + WOFF + (8 * g + q) * WROW + (p >> 1) * 16 + (p & 1) * 8 + (tile(i) ^ (FFN ? g & 1 : 2 * (g & 1) + (q >> 1))) * 32;
  const unsigned scr = kScr + w * kScrW;
  unsigned ast, acx[NT], abi[NT], sto, sso[NT];
  unsigned co[3], ce[2], ak0, ak1, av0, av1, oso;
  f32x2 kb[4];
  u32x4 one = {0x3f803f80u, 0x3f803f80u, 0x3f803f80u, 0x3f803f80u};
  OPAQUE(one);
  const char* first;
  auto setup = [&]() {
    p2.init(lane, w, ws);
    ast = scr + m * 32; OPAQUE(ast);
#pragma unroll
    for (int n = 0; n < NT; ++n) { acx[n] = kCex + (tile(n) * 16 + 4 * g) * 4; OPAQUE(acx[n]); }
    if (FFN) {
      sto = (g & 1) * 1024 + ((g >> 1) * 16 + m) * 16; OPAQUE(sto);
#pragma unroll
      for (int n = 0; n < NT; ++n) {
        abi[n] = scr + 1024 + (tile(n) - t0) * 32 + g * 8; OPAQUE(abi[n]);
        sso[n] = ((2 * rt + mh) * 72 + 7 * cg + (tile(n) >> 1)) * 4096 + (tile(n) & 1) * 512; OPAQUE_S(sso[n]);
      }
      const int l0 = ((rt * 8 + cg) * 8 + w) * 16;
      first = (l0 < 12544 ? p2.w2 : p2.wo) + (l0 < 12544 ? l0 : l0 < 15680 ? l0 - 12544 : 0) * 128;
      OPAQUE_S(first);
    }
    PIN();
  };
  auto prep = [&](int i) {
    if (FFN) return;
    const int blk = w >> 1, par = w & 1;
    const unsigned rb = kAtt + (par * 72 + 16 * blk) * 400;
    if (i < 3) {
      const int T = tile(i), n = 16 * (T & 3) + 4 * g;
      const unsigned c = kAtt + (T >> 2) * 128 + (n + (n < 28 ? 0 : n < 56 ? 4 : n < 60 ? -28 : 0)) * 2;
      co[i] = c + (8 + 32 * mh + m) * 400; OPAQUE(co[i]);
      if (i < 2) { ce[i] = c + ((m & 1) * 72 + (m >> 1)) * 400; OPAQUE(ce[i]); }
    } else if (i == 3) { ak0 = rb + m * 400 + 16 * g; ak1 = rb + (16 + (m & 7)) * 400 + 128 + 16 * g; OPAQUE(ak0); OPAQUE(ak1); }
    else if (i == 4) { av0 = rb + (4 * g + q) * 400 + 256 + 16 * p; av1 = rb + (16 + 4 * (g & 1) + q) * 400 + 256 + 16 * p; OPAQUE(av0); OPAQUE(av1); }
    else if (i < 9) {
      const int k = i - 5, d = m + 8 - 4 * g - (16 * (k >> 1) + 2 * (k & 1));
      kb[k] = f32x2{(unsigned)d < 8u ? 0.f : -__builtin_inff(), (unsigned)(d - 1) < 8u ? 0.f : -__builtin_inff()};
      OPAQUE(kb[k]);
    }
  };
  oso = (((2 * rt + (w >> 2)) * 72 + 56 + 2 * cg) * 4 + (w & 3)) * 1024;
  const unsigned tag = (unsigned)tag64;
  f32x4 acc[4][NT], ace[2];
  bf16x8 X[2][4], W[2][NT], XE[2];
  constexpr int NRD = NT + 2 + (FFN ? 0 : 1);
  auto rd1 = [&](int kk, int i) {
    if (i == 0) { X[kk][0] = lds_b128(ax[kk]); X[kk][1] = lds_b128(ax[kk] + 2048); }
    else if (i == 2) { X[kk][2] = lds_b128(ax[kk] + 4096); X[kk][3] = lds_b128(ax[kk] + 6144); }
    else if (i < NT + 2) { const int n = i == 1 ? 0 : i - 2; W[kk][n] = lds_tr(aw[n] + kk * 32 * WROW, aw[n] + (kk * 32 + 4) * WROW); }
    else if (i < NRD) XE[kk] = lds_b128(ae[kk]);
  };
  auto half = [&](int kk, bool first, int nex, bool reads, int adv, auto load_at, unsigned slot, auto hook) {
    u32x4 e = one;
#pragma unroll
    for (int i = 0; i < 4 * NT + 4; ++i) {
      if (i >= 4 * NT + nex) break;
      const int l = load_at(i);
      if (l >= 100) { if ((l & 3) == 0) p2.m0((l - 100) >> 2); } else if (l >= 0) ld_m0(l, slot);
      if (!FFN && i == 4 * NT + 2) { e = m == 0 ? one : __builtin_bit_cast(u32x4, XE[1 - kk]); OPAQUE(e); NOP(1); }
      const int mt = (i & 4) ? 3 - (i & 3) : i & 3;
      if (i < 4 * NT) mma(acc[mt][i >> 2], W[kk][i >> 2], X[kk][mt], first);
      else mma(ace[i & 1], W[i < 4 * NT + 2 ? kk : 1 - kk][i & 1], e, first);
      if (l >= 100) p2.go((l - 100) >> 2, l & 3); else if (l >= 0) ld_go(l);
      if (reads && i % 2 == 0 && i / 2 < NRD) {
        const int r = i / 2;
        if (adv) {
          if (r == 0) { ax[0] += adv; OPAQUE(ax[0]); }
          else if (r == 2) { ax[1] += adv; OPAQUE(ax[1]); }
          else if (r < NT + 2) { const int n = r == 1 ? 0 : r - 2; aw[n] += adv; OPAQUE(aw[n]); }
          else { ae[0] += adv; ae[1] += adv; OPAQUE(ae[0]); OPAQUE(ae[1]); }
        }
        rd1(1 - kk, r);
      }
      if (!FFN && nex && i == 4 * NT - 3) { e = m == 0 ? one : __builtin_bit_cast(u32x4, XE[kk]); OPAQUE(e); }
      hook(i);
      PIN();
    }
  };
  auto ldstats = [&](int i) {
    if (i == 0) dma_dev(lane * 16, ws + kWsStats + (r0 + 64 * mh) * 16, scr, ~0ull);
    else if (FFN) dma_dev(lane * 16, b1 + (cg * 14 + t0) * 32, scr + 1024, (1ull << (2 * NT)) - 1);
    else dma_dev(rt == 0 ? 0 : lane * 16, ws + kWsStats + (rt == 0 ? 0 : r0 - 16) * 16, scr + 1024, 0xffffull);
  };
  u32x4 st[5];
  u32x2 bias[NT];
  f32x4 cs[NT];
  unsigned bad = 0;
  auto rdstats = [&](int i) { st[i] = *(LAS u32x4*)(size_t)(i < 4 ? ast + (i >> 1) * 512 + (i & 1) * 16 : scr + 1024 + m * 16); };
  auto check = [&](int i) { bad |= (st[i][1] ^ tag) | (st[i][3] ^ tag); OPAQUE(bad); };
  constexpr int NST = FFN ? 4 : 5;

  WAIT_VM(8);
  BARRIER();
#pragma unroll
  for (int i = 0; i < NRD; ++i) rd1(0, i);
  sg.next();
#pragma unroll
  for (int l = 0; l < 3; ++l) { ld_m0(l, slot_of(2)); NOP(0); ld_go(l); }
  PIN();
#pragma unroll
  for (int s = 0; s < 6; ++s) {
    auto three = [](int l0) { return [=](int i) { return i % 4 == 1 && i < 12 ? l0 + i / 4 : -1; }; };
    auto four = [](int l0) { return [=](int i) { return i % 3 == 1 && i < 12 ? l0 + i / 3 : -1; }; };
    auto none = [](int) { return -1; };
    if (s < 5) half(0, s == 0, 2, true, 0, three(3), slot_of(s + 2), [&](int i) { if (s == 3 && (i == 11 || i == 13)) ldstats(i == 13); });
    else half(0, false, 2, true, 0, none, 0, [&](int i) {
      if ((i & 1) && i / 2 < NST) rdstats(i / 2);
      if (i >= 9 && i < 9 + NST) check(i - 9);
    });
    if (s == 0) setup();
    if (FFN && s == 2) touch(lane * 128, first, scr + 1280, 0xffff);
    if (s == 5 && __builtin_expect(__ballot(bad != 0) != 0, 0)) {
#pragma nounroll
      for (int spin = 0; spin < kSpinCap; ++spin) {
        __builtin_amdgcn_s_sleep(2);
        ldstats(0);
        if (!FFN) ldstats(1);
        WAIT_VM(0);
        bad = 0;
#pragma unroll
        for (int i = 0; i < NST; ++i) { rdstats(i); check(i); }
        if (__ballot(bad != 0) == 0) break;
      }
    }
    WAIT_LGKM0();
    if (s < 5) WAIT_VM(6 + (FFN && s == 2) + (s == 3 ? 2 : 0));
    else WAIT_VM(4);
    BARRIER();
    if (s < 4) sg.next();
    PIN();
    if (s < 4) half(1, false, 2, true, slot_of(s + 1) - slot_of(s), three(0), slot_of(s), [&](int i) { if (s >= 1 && i % 4 == 3) prep(3 * (s - 1) + i / 4); });
    else half(1, false, 2, true, slot_of(s + 1) - slot_of(s), four(100 + 4 * (s - 4)), 0, [&](int) {});
  }
  auto blk4 = [&](int kk, int n, auto hook) {
#pragma unroll
    for (int mt = 0; mt < 4; ++mt) { mma(acc[mt][n], W[kk][n], X[kk][mt], false); hook(mt); PIN(); }
  };
  {
    u32x4 e0 = one, e1 = one;
    blk4(0, 0, [&](int i) { rd1(1, i == 0 ? 1 : i == 1 ? 3 : i == 2 ? (FFN ? 0 : NRD - 1) : (FFN ? 2 : 0)); });
    if (!FFN) { e0 = m == 0 ? one : __builtin_bit_cast(u32x4, XE[0]); OPAQUE(e0); }
    blk4(0, 1, [&](int i) {
      if (FFN) { if (i + 4 < NRD) rd1(1, i + 4); bias[i < NT ? i : 0] = *(LAS u32x2*)(size_t)abi[i < NT ? i : 0]; }
      else if (i < 2) rd1(1, i == 0 ? 2 : 4);
    });
    mma(ace[0], W[0][0], e0, false); mma(ace[1], W[0][1], e0, false);
    if (!FFN) { e1 = m == 0 ? one : __builtin_bit_cast(u32x4, XE[1]); OPAQUE(e1); NOP(1); }
    mma(ace[0], W[1][0], e1, false); mma(ace[1], W[1][1], e1, false);
    PIN();
    blk4(0, 2, [&](int) {});
    NOP(3);
    asm volatile("s_mov_b64 exec, %4\n\tds_write_b128 %0, %1\n\tds_write_b128 %2, %3\n\ts_mov_b64 exec, -1\n\ts_waitcnt lgkmcnt(0)"
                 ::"v"(acx[0]), "v"(ace[0]), "v"(acx[1]), "v"(ace[1]), "s"(0x0001000100010001ull) : "memory");
  }
  WAIT_LGKM0();
  BARRIER();
  p2.m0(2);
  blk4(1, 0, [&](int i) { p2.go(2, i); if (i < NT) cs[i] = lds_f4(acx[i]); });
  blk4(1, 1, [&](int) {});
  float rs[5], mu[5];
#pragma unroll
  for (int i = 0; i < NST; ++i) { rs[i] = __uint_as_float(st[i][2]); mu[i] = FFN ? -rs[i] * __uint_as_float(st[i][0]) : -__uint_as_float(st[i][0]); }
  auto epi = [&](int n) {
    if (FFN) {
      const f32x2 b0 = {blo(bias[n][0]), bhi(bias[n][0])}, b1v = {blo(bias[n][1]), bhi(bias[n][1])};
#pragma unroll
      for (int pp = 0; pp < 2; ++pp) {
        unsigned y[2][2];
#pragma unroll
        for (int e = 0; e < 2; ++e) {
          const int mt = 2 * pp + e;
          f32x2 u0 = fma2(rs[mt], lo2(acc[mt][n]), fma2(mu[mt], lo2(cs[n]), b0)), u1 = fma2(rs[mt], hi2(acc[mt][n]), fma2(mu[mt], hi2(cs[n]), b1v));
          u0 = f32x2{fmaxf(u0[0], 0.f), fmaxf(u0[1], 0.f)}; u1 = f32x2{fmaxf(u1[0], 0.f), fmaxf(u1[1], 0.f)};
          y[e][0] = pack2(u0 * u0); y[e][1] = pack2(u1 * u1);
        }
        const auto s0 = __builtin_amdgcn_permlane16_swap(y[0][0], y[1][0], false, false);
        const auto s1 = __builtin_amdgcn_permlane16_swap(y[0][1], y[1][1], false, false);
        store_wt(sto, p2.sa, sso[n], pp * 2048, u32x4{s0[0], s1[0], s0[1], s1[1]});
      }
    } else {
      auto put = [&](const f32x4& a, int i, unsigned addr) {
        *(LAS u32x2*)(size_t)addr = u32x2{pack2(fma2(mu[i], lo2(cs[n]), lo2(a)) * rs[i]), pack2(fma2(mu[i], hi2(cs[n]), hi2(a)) * rs[i])};
      };
      if (n < 2) put(ace[n], 4, ce[n]);
#pragma unroll
      for (int mt = 0; mt < 4; ++mt) put(acc[mt][n], mt, co[n] + ((mt & 1) * 72 + 16 * (mt >> 1)) * 400);
    }
    PIN();
  };
  PIN();
  NOP(3);
  PIN();
  epi(0);
  blk4(1, 2, [&](int) {});
  epi(1);
  if (NT == 4) { blk4(0, 3, [&](int) {}); blk4(1, 3, [&](int) {}); }
  epi(2);
  if (NT == 4) epi(3);
  p2.init2(lane, w, kWsFlags + ((rt * 16 + cg + (FFN ? 0 : 8)) * 2 + (FFN ? w & 1 : w >> 2)) * 128 + (FFN ? w >> 1 : w & 3) * 8);
  if (FFN) {
    WAIT_VM(0);
    store_flag(p2.flag, ws, tag64);
    p2.rd(0);
    p2.rd(1);
    p2.rd(2);
    WAIT_LGKM0();
    BARRIER();
    p2.ld(3); p2.ld(4); p2.ld(5);
    p2.ld(6);
    WAIT_VM(12); p2.rd(3); WAIT_LGKM0();
    p2.ld(7);
    WAIT_VM(12); p2.rd(4);
    p2.peek(ws);
    WAIT_LGKM0();
    p2.ld(8);
  } else {
    WAIT_VM(4);
    p2.rd(0);
    p2.rd(1);
    WAIT_LGKM0();
    BARRIER();
    p2.ld(3);
    const f32x4 z = {0.f, 0.f, 0.f, 0.f};
    f32x4 sc[2][2];
    bf16x8 qf[2], kf[2][2], vf[2][2];
#pragma unroll
    for (int hd = 0; hd < 2; ++hd) { qf[hd] = lds_b128(ak0 + 8 * 400 + hd * 64); kf[hd][0] = lds_b128(ak0 + 128 + hd * 64); kf[hd][1] = lds_b128(ak1 + hd * 64); }
#pragma unroll
    for (int hd = 0; hd < 2; ++hd) { vf[hd][0] = lds_tr(av0 + hd * 64, av1 + hd * 64); vf[hd][1] = lds_tr(av0 + hd * 64 + 8, av1 + hd * 64 + 8); }
    PIN();
#pragma unroll
    for (int hd = 0; hd < 2; ++hd) { sc[hd][0] = mfma(kf[hd][0], qf[hd], z); sc[hd][1] = mfma(kf[hd][1], qf[hd], z); }
    PIN();
    p2.ld(4);
    WAIT_VM(8);
    p2.rd(2);
    WAIT_LGKM0();
    BARRIER();
    p2.ld(5);
    PIN();
    f32x2 e[2][4];
    float mx[2], sum[2];
    const float kLog = 0.18898223650461362f * 1.4426950408889634f;
#pragma unroll
    for (int hd = 0; hd < 2; ++hd) {
#pragma unroll
      for (int t = 0; t < 2; ++t) {
        e[hd][2 * t] = fma2(kLog, lo2(sc[hd][t]), kb[2 * t]);
        e[hd][2 * t + 1] = fma2(kLog, hi2(sc[hd][t]), kb[2 * t + 1]);
      }
    }
    if (__builtin_expect(r0 + 32 * (w >> 1) == 0, 0)) {
#pragma unroll
      for (int i = 0; i < 2; ++i) {
        const f32x2 v = kb[i] + -1.0e9f * 1.4426950408889634f;
        if (g < 2) e[0][i] = e[1][i] = v;
      }
    }
#pragma unroll
    for (int hd = 0; hd < 2; ++hd) {
      mx[hd] = max3_pad(max3(e[hd][0][0], e[hd][0][1], e[hd][1][0]), max3(e[hd][1][1], e[hd][2][0], e[hd][2][1]), max3(e[hd][3][0], e[hd][3][1], e[hd][3][1]));
    }
    rows2(mx[0], mx[1], [](float a, float b) { return max3_pad(a, b, b); });
    PIN();
    p2.ld(6);
    PIN();
#pragma unroll
    for (int hd = 0; hd < 2; ++hd) {
#pragma unroll
      for (int i = 0; i < 4; ++i) { const f32x2 d = e[hd][i] - mx[hd]; e[hd][i] = f32x2{__builtin_amdgcn_exp2f(d[0]), __builtin_amdgcn_exp2f(d[1])}; }
      const f32x2 t = (e[hd][0] + e[hd][1]) + (e[hd][2] + e[hd][3]);
      sum[hd] = t[0] + t[1];
    }
    rows2(sum[0], sum[1], [](float a, float b) { return a + b; });
    f32x4 o[2][2];
#pragma unroll
    for (int hd = 0; hd < 2; ++hd) {
      const float inv = __builtin_amdgcn_rcpf(sum[hd]);
      const u32x4 pf = {pack2(e[hd][0] * inv), pack2(e[hd][1] * inv), pack2(e[hd][2] * inv), pack2(e[hd][3] * inv)};
      o[hd][0] = mfma(vf[hd][0], __builtin_bit_cast(bf16x8, pf), z);
      o[hd][1] = mfma(vf[hd][1], __builtin_bit_cast(bf16x8, pf), z);
    }
    WAIT_VM(8);
    p2.rd(3);
    p2.rd(4);
#pragma unroll
    for (int hd = 0; hd < 2; ++hd)
      store_wt(lane * 16, p2.sa, oso + hd * 4096, 0, u32x4{pack2(lo2(o[hd][0])), pack2(hi2(o[hd][0])), pack2(lo2(o[hd][1])), pack2(hi2(o[hd][1]))});
    WAIT_LGKM0();
    p2.ld(7);
    WAIT_VM(4);
    store_flag(p2.flag, ws, tag64);
    p2.peek(ws);
    p2.ld(8);
  }
}

__global__ __launch_bounds__(512, 2) void fused_kernel(const char*, const char*, const char*, const char*, const char*, const char*, const char*, char*, char*) {
  u32x16 ka;
  u64 kb;
  asm volatile("s_load_dwordx16 %0, %2, 0x0\n\ts_load_dwordx2 %1, %2, 0x40" : "=&s"(ka), "=&s"(kb) : "s"(__builtin_amdgcn_kernarg_segment_ptr()));
  __shared__ __attribute__((aligned(16))) char smem[kLds];
  asm volatile("" ::"v"((LAS char*)smem));
  const int lane = threadIdx.x & 63, w = __builtin_amdgcn_readfirstlane(threadIdx.x >> 6);
  const int bid = blockIdx.x, xcd = bid & 7, j = bid >> 3;
  const int rt = (xcd & 1) * 8 + (j & 7), cgi = j >> 3, cg = 2 * (xcd >> 1) + (cgi & 1);
  const bool ffn = cgi < 2;
  const char *x, *wo, *b1, *w2, *b2;
  char *out, *ws;
  Stg sg;
  u64 tag64;
  {
    const int r0 = rt * 128;
#pragma unroll
    for (int u = 0; u < 2; ++u) {
      const unsigned r = 8 * u + (lane >> 3);
      sg.vo[u] = __umul24(r0 + 32 * (w >> 1) + 2 * r + (w & 1), 896) + (((lane & 7) ^ (r >> 1)) << 4) - u * 1024;
    }
    sg.dx = w * 2048;
    sg.ks = mod7(mod7((rt & 3) * 2 + 2 * (xcd >> 1)));
    asm volatile("" : "+v"(sg.vo[0]), "+v"(sg.vo[1]), "+s"(sg.ks), "+s"(sg.dx));
    asm volatile("s_waitcnt lgkmcnt(0)" : "+s"(ka), "+s"(kb));
    auto ptr = [&](int i) { return (char*)(((u64)ka[2 * i + 1] << 32) | ka[2 * i]); };
    x = ptr(0); wo = ptr(2); b1 = ptr(4); w2 = ptr(5); b2 = ptr(6); out = ptr(7); ws = (char*)kb;
    sg.xs = make_srd(x, 2048 * 896);
    set_m0(slot_of(0), sg.dx);
    NOP(0);
    dma(sg.vo[0], sg.xs, sg.ks * 128, 0);
    dma(sg.vo[1], sg.xs, sg.ks * 128, 1024);
    PIN();
    const unsigned q0 = 48 * w + (lane >> 2), l3 = (lane & 3) << 4;
    if (ffn) {
      auto woff = [&](unsigned Q) { const unsigned k = __umul24(Q, 9363) >> 16; return __umul24(k, 3584 - 448) + Q * 64 + cg * 448 + (l3 ^ ((k << 2) & 32)); };
#pragma unroll
      for (int u = 0; u < 3; ++u) sg.vo[3 + u] = woff(q0 + 16 * u) - u * 1024;
      sg.vo[2] = woff(384 + 8 * w + ((lane >> 2) & 7));
    } else {
#pragma unroll
      for (int u = 0; u < 3; ++u) {
        const unsigned Q = q0 + 16 * u, k = __umul24(Q, 10923) >> 16, cq = Q - 6 * k, c = (((cq & 1) << 6) | l3) ^ ((k & 2) << 4) ^ ((k & 8) << 3);
        sg.vo[3 + u] = (c == 112 ? kOut : __umul24(k, 2688) + __umul24(cq >> 1, 896) + cg * 112 + c) - u * 1024;
      }
      const int hr = r0 - 16 + 2 * w + ((lane >> 3) & 1);
      sg.vo[2] = __umul24(hr < 0 ? 0 : hr, 896) + (((lane & 7) ^ w) << 4);
    }
    sg.dw = (ffn ? 16384 : 18432) + w * 3072;
    sg.dp = ffn ? 16384 + 24576 + w * 512 : 16384 + w * 256;
    sg.kstep = ffn ? 64 * 3584 : 64 * 2688;
    sg.ws = make_srd(ffn ? ptr(3) : ptr(1), 7 * sg.kstep);
    const u32x4 ps = ffn ? sg.ws : sg.xs;
    const u64 part = ffn ? 0xffffffffull : 0xffffull;
    auto ld = [&](int s, int l) {
      if (l == 0 || l == 2 || l == 3) set_m0(slot_of(s), l == 0 ? sg.dx : l == 2 ? sg.dp : sg.dw);
      if (l == 0 || l == 3) NOP(0);
      if (l < 2) dma(sg.vo[l], sg.xs, sg.xo, l * 1024);
      else if (l == 2) dma_part(sg.vo[2], ps, ffn ? sg.wo : sg.xo, part);
      else dma(sg.vo[l], sg.ws, sg.wo, (l - 3) * 1024);
    };
    const int strow = (xcd & 1) * 1024 + (xcd >> 1) * 256 + j * 8 + w;
    u32x2 ep;
    u32x4 srow;
    asm volatile("s_mov_b64 exec, 1\n\tglobal_load_dwordx2 %0, %1, %2 sc1\n\ts_mov_b64 exec, -1" : "=v"(ep) : "v"(0), "s"(ws) : "memory");
    asm volatile("buffer_load_dwordx4 %0, %1, %2, %3 offen" : "=v"(srow) : "v"(lane < 56 ? lane * 16 : kOut), "s"(sg.xs), "s"(strow * 896) : "memory");
    sg.next();
    ld(0, 3); ld(0, 4); ld(0, 5); ld(0, 2);
    {
      const char* pc;
      unsigned nl;
      asm volatile("s_getpc_b64 %0\n.Lfk_pc:\n\ts_mov_b32 %1, (.Lfk_end - .Lfk_pc) / 128" : "=s"(pc), "=s"(nl));
      const unsigned li = (j * 8 + w) * 2;
      touch((li + lane) * 128, pc, kScr + w * kScrW + 1280, (1u << (min(nl, li + 2) - min(nl, li))) - 1);
    }
    asm volatile("s_waitcnt vmcnt(5)" : "+v"(srow), "+v"(ep)::"memory");
    tag64 = (((u64)__builtin_amdgcn_readfirstlane(ep[1]) << 32) | (unsigned)__builtin_amdgcn_readfirstlane(ep[0])) + 1;
    float sm = 0.f, sq = 0.f;
#pragma unroll
    for (int i = 0; i < 4; ++i) {
      const unsigned u = srow[i];
      const bf16x2 v = __builtin_bit_cast(bf16x2, u);
      sm = __builtin_amdgcn_fdot2_f32_bf16(v, __builtin_bit_cast(bf16x2, 0x3f803f80u), sm, false);
      sq = __builtin_amdgcn_fdot2_f32_bf16(v, v, sq, false);
    }
    sm += dpp<0xB1>(sm); sq += dpp<0xB1>(sq);
    sm += dpp<0x4E>(sm); sq += dpp<0x4E>(sq);
    sm += dpp<0x141>(sm); sq += dpp<0x141>(sq);
    sm += dpp<0x140>(sm); sq += dpp<0x140>(sq);
    rows2(sm, sq, [](float a, float b) { return a + b; });
    sm *= 1.f / 448.f;
    store_wt_lane0(kWsStats + strow * 16, ws,
                   u32x4{__float_as_uint(sm), (unsigned)tag64, __float_as_uint(__builtin_amdgcn_rsqf(fmaxf(fmaf(-sm, sm, sq * (1.f / 448.f)), 0.f) + 1e-5f)), (unsigned)tag64});
    sg.next();
#pragma unroll
    for (int l = 0; l < 6; ++l) ld(1, l);
  }
  const int rg = 4 * xcd + (j & 3), ct = mod7(mod7(7 + (j < 28 ? j >> 2 : 6) - 2 * (j & 3)));
  const int m = lane & 15, g = lane >> 4;
  P2 p2;
  p2.sA0 = w * 4096; p2.sA2 = (w < 3 ? 32768 : 65536) + w * 4096; p2.rot = 2 * (rg & 3);
  p2.w2 = w2; p2.wo = wo; p2.rg = rg; p2.ct = ct; p2.live = j < 28;

  if (!ffn) phase1<false, 3>(sg, b1, ws, tag64, lane, w, rt, cg, p2);
  else if (w < 4) phase1<true, 4>(sg, b1, ws, tag64, lane, w, rt, cg, p2);
  else phase1<true, 3>(sg, b1, ws, tag64, lane, w, rt, cg, p2);

  if (j >= 28) {
    if (bid == 255) {
      const unsigned t0 = (unsigned)tag64, t1 = (unsigned)(tag64 >> 32);
#pragma nounroll
      for (int spin = 0; spin < kSpinCap; ++spin) {
        u32x4 f, h;
        asm volatile("global_load_dwordx4 %0, %2, %3 sc1\n\tglobal_load_dwordx4 %1, %2, %4 sc1\n\ts_waitcnt vmcnt(0)" : "=&v"(f), "=&v"(h)
                     : "v"((lane >> 1) * 128 + (lane & 1) * 16), "s"(ws + kWsFlags + w * 8192), "s"(ws + kWsFlags + w * 8192 + 4096) : "memory");
        if (__all(f[0] == t0 && f[1] == t1 && f[2] == t0 && f[3] == t1 && h[0] == t0 && h[1] == t1 && h[2] == t0 && h[3] == t1)) break;
        __builtin_amdgcn_s_sleep(8);
      }
      BARRIER();
      if (w == 0) store_flag(0, ws, tag64);
    }
    return;
  }

  const unsigned fofs = kWsFlags + (((rg >> 1) * 16 + w) * 2 + (rg & 1)) * 128 + (lane & 3) * 8;
  auto ready = [&](u32x2 f) { return (__ballot(f[0] == (unsigned)tag64 && f[1] == (unsigned)(tag64 >> 32)) & 15) == 15; };
  auto poll = [&](unsigned ofs) {
#pragma nounroll
    for (int spin = 0; spin < kSpinCap; ++spin) {
      u32x2 f;
      asm volatile("s_mov_b64 exec, 15\n\tglobal_load_dwordx2 %0, %1, %2 sc1\n\ts_mov_b64 exec, -1\n\ts_waitcnt vmcnt(0)" : "=&v"(f) : "v"(ofs), "s"(ws) : "memory");
      if (ready(f)) break;
      __builtin_amdgcn_s_sleep(1);
    }
  };
  BARRIER();
  WAIT_VM(4);
  if (!ready(*(LAS u32x2*)(size_t)(kScr + 1408 + w * kScrW + (lane & 3) * 8))) poll(fofs);
  u32x4 H[WIN][4];
  auto hso = [&](int i) { return (unsigned)(rg * 72 + (i < 7 ? 7 * w + p2.kstep(i) : 56 + 2 * w + (i - 7))) * 4096; };
  auto ldh = [&](int i, int mt, unsigned so) {
    asm volatile("buffer_load_dwordx4 %0, %1, %2, %3 offen offset:%4 sc1" : "=v"(H[i % WIN][mt]) : "v"(lane * 16), "s"(p2.sa), "s"(so), "i"(mt * 1024) : "memory");
  };
  asm volatile("s_cmp_lt_u32 %0, 4\n\ts_cbranch_scc1 1f\n\ts_setprio 1\n1:" ::"s"(w) : "scc");
#pragma unroll
  for (int i = 0; i < WIN; ++i) {
    const unsigned so = hso(i);
#pragma unroll
    for (int mt = 0; mt < 4; ++mt) ldh(i, mt, so);
  }
  const int os = threadIdx.x >> 3, orow = rg * 64 + 32 * (os >> 5) + 2 * (os & 15) + ((os >> 4) & 1);
  unsigned oofs = __umul24(orow, 896) + ct * 128 + (threadIdx.x & 7) * 16;
  u32x4 xr, br;
  unsigned pa[3][4], ra[2][2];
#pragma unroll
  for (int u = 0; u < 4; ++u) {
    const unsigned a = m * 256 + (((4 * u + g) ^ (m & 7)) << 4);
    pa[0][u] = a + p2.sA0; pa[1][u] = a + p2.sA0 + 98304; pa[2][u] = a + p2.sA2;
    OPAQUE(pa[0][u]); OPAQUE(pa[1][u]); OPAQUE(pa[2][u]);
  }
  ra[0][0] = (w < 2 ? 0 : w < 4 ? kSlot : w < 6 ? 98304 : 32768) + (os & 15) * 256 + (((2 * (threadIdx.x & 7)) ^ (os & 7)) << 4);
  ra[0][1] = ra[0][0] ^ 16;
  ra[1][0] = ra[0][0] + (w < 6 ? 0 : 32768);
  ra[1][1] = ra[1][0] ^ 16;
  OPAQUE(ra[0][0]); OPAQUE(ra[0][1]); OPAQUE(ra[1][0]); OPAQUE(ra[1][1]); OPAQUE(oofs);
  f32x4 acc[4][4], lo, hi;
  u32x2 fq;
  auto park = [&](int mt, int u) { *(LAS f32x4*)(size_t)(mt < 2 ? pa[0][u] + mt * kSlot : pa[mt - 1][u]) = acc[mt][u]; };
  auto ldfq = [&]() { asm volatile("s_mov_b64 exec, 15\n\tglobal_load_dwordx2 %0, %1, %2 offset:2048 sc1\n\ts_mov_b64 exec, -1" : "=v"(fq) : "v"(fofs), "s"(ws) : "memory"); };
  auto ldxb = [&]() {
    asm volatile("global_load_dwordx4 %0, %1, %2" : "=v"(xr) : "v"(oofs), "s"(x) : "memory");
    asm volatile("global_load_dwordx4 %0, %1, %2" : "=v"(br) : "v"(ct * 128 + (threadIdx.x & 7) * 16), "s"(b2) : "memory");
  };
  static_for<0, 9>([&](auto I) {
    constexpr int i = I.v;
    const unsigned so = i + WIN < 9 ? hso(i + WIN) : 0;
    if (i == 7 - WIN) {
      asm volatile("s_waitcnt vmcnt(6)" : "+v"(fq)::"memory");
      if (!ready(fq)) poll(fofs + 2048);
    }
    static_for<0, 4>([&](auto MT) {
      constexpr int mt = MT.v, left = 4 * (8 - i) + 3 - mt;
      constexpr int younger = (left < 4 * WIN - 1 ? left : 4 * WIN - 1);
      asm volatile("s_waitcnt vmcnt(%1)" : "+v"(H[i % WIN][mt]) : "i"(younger) : "memory");
      static_for<0, 4>([&](auto UU) {
        constexpr int uu = UU.v, u = i < 8 && (mt & 1) ? 3 - uu : uu;
        mma(acc[mt][u], p2.Wf[i % 5][u], H[i % WIN][mt], i == 0);
        if (uu == 0 && i >= 1 && i < 3) p2.rd1(i + 4, mt);
        if (uu == 0 && i >= 4 && i < 6) p2.rd1(i + 3, mt);
        if (i == 7 && mt == 0 && uu == 0) {
          OPAQUE(xr); OPAQUE(br);
          lo = f32x4{blo(xr[0]) + blo(br[0]), bhi(xr[0]) + bhi(br[0]), blo(xr[1]) + blo(br[1]), bhi(xr[1]) + bhi(br[1])};
          hi = f32x4{blo(xr[2]) + blo(br[2]), bhi(xr[2]) + bhi(br[2]), blo(xr[3]) + blo(br[3]), bhi(xr[3]) + bhi(br[3])};
          OPAQUE(lo); OPAQUE(hi);
        }
        if (i == 8 && mt >= 1) park(mt >= 1 ? mt - 1 : 0, u);
        PIN();
      });
      if (i + WIN < 9) ldh(i, mt, so);
    });
    if (i == 5 - WIN) ldfq();
    if (i == 6 - WIN) ldxb();
  });
  NOP(6);
  PIN();
#pragma unroll
  for (int u = 0; u < 4; ++u) park(3, u);
  WAIT_LGKM0();
  BARRIER();
#pragma unroll
  for (int k = 0; k < 8; k += 2) {
    lo += lds_f4(ra[k >= 3][0] + k * 4096) + lds_f4(ra[k + 1 >= 3][0] + (k + 1) * 4096);
    hi += lds_f4(ra[k >= 3][1] + k * 4096) + lds_f4(ra[k + 1 >= 3][1] + (k + 1) * 4096);
  }
  *(__attribute__((address_space(1))) u32x4*)(out + oofs) = u32x4{pack2(lo2(lo)), pack2(hi2(lo)), pack2(lo2(hi)), pack2(hi2(hi))};
  asm volatile(".Lfk_end:");
}

extern "C" void kernel_launch(void* const* d_in, const int* in_sizes, int n_in, void* d_out, int out_size, void* d_ws, size_t ws_size, hipStream_t stream) {
  fused_kernel<<<256, 512, 0, stream>>>((const char*)d_in[0], (const char*)d_in[1], (const char*)d_in[2], (const char*)d_in[4], (const char*)d_in[5],
                                        (const char*)d_in[6], (const char*)d_in[7], (char*)d_out, (char*)d_ws);
}
```

```cpp
#include <hip/hip_runtime.h>
#include <hip/hip_bf16.h>

typedef __bf16 bf16x2 __attribute__((ext_vector_type(2)));
typedef __bf16 bf16x4 __attribute__((ext_vector_type(4)));
typedef __bf16 bf16x8 __attribute__((ext_vector_type(8)));
typedef float f32x2 __attribute__((ext_vector_type(2)));
typedef float f32x4 __attribute__((ext_vector_type(4)));
typedef unsigned u32x2 __attribute__((ext_vector_type(2)));
typedef unsigned u32x4 __attribute__((ext_vector_type(4)));
typedef unsigned u32x16 __attribute__((ext_vector_type(16)));
typedef unsigned long long u64;
#define LAS __attribute__((address_space(3)))
#define DEV __device__ __forceinline__

constexpr int kLds = 163840;
constexpr int kSlot = 45056;
constexpr int kCex = kLds - 1024;
constexpr int kAtt = kCex - 144 * 400;
constexpr int kScr = 3 * kSlot, kScrW = 1536;
constexpr int kWsFlags = 4096, kWsStats = 131072, kWsA2 = 262144;
constexpr int kSpinCap = 1 << 16;
constexpr int WIN = 2;
constexpr unsigned kOut = 0x80000000u;

#define PIN() __builtin_amdgcn_sched_barrier(0)
#define OPAQUE(x) asm volatile("" : "+v"(x))
#define OPAQUE_S(x) asm volatile("" : "+s"(x))
#define NOP(n) asm volatile("s_nop %0" ::"i"(n))
#define WAIT_VM(n) asm volatile("s_waitcnt vmcnt(%0)" ::"i"(n) : "memory")
#define WAIT_LGKM0() do { asm volatile("" ::: "memory"); __builtin_amdgcn_s_waitcnt(0xC07F); asm volatile("" ::: "memory"); } while (0)
#define BARRIER() do { asm volatile("" ::: "memory"); __builtin_amdgcn_s_barrier(); asm volatile("" ::: "memory"); } while (0)

DEV u32x4 make_srd(const char* p, unsigned bytes) { return u32x4{(unsigned)(u64)p, (unsigned)((u64)p >> 32) & 0xffffu, bytes, 0x00020000u}; }
DEV void set_m0(unsigned a, unsigned b) {
  if (__builtin_constant_p(a)) asm volatile("s_add_u32 m0, %1, %0" ::"i"(a), "s"(b) : "scc");
  else asm volatile("s_add_u32 m0, %0, %1" ::"s"(a), "s"(b) : "scc");
}
DEV void dma(unsigned voff, u32x4 srd, unsigned soff, int ofs) {
  asm volatile("buffer_load_dwordx4 %0, %1, %2 offen offset:%3 lds" ::"v"(voff), "s"(srd), "s"(soff), "i"(ofs) : "memory");
}
DEV void dma_part(unsigned voff, u32x4 srd, unsigned soff, u64 lanes) {
  asm volatile("s_mov_b64 exec, %3\n\tbuffer_load_dwordx4 %0, %1, %2 offen lds\n\ts_mov_b64 exec, -1" ::"v"(voff), "s"(srd), "s"(soff), "s"(lanes) : "memory");
}
DEV void dma_dev(unsigned voff, const char* sbase, unsigned dst, u64 lanes) {
  asm volatile("s_mov_b32 m0, %2\n\ts_mov_b64 exec, %3\n\tglobal_load_lds_dwordx4 %0, %1 sc1\n\ts_mov_b64 exec, -1" ::"v"(voff), "s"(sbase), "s"(dst), "s"(lanes) : "memory");
}
DEV void touch(unsigned voff, const char* sbase, unsigned dst, u64 lanes) {
  asm volatile("s_mov_b32 m0, %2\n\ts_mov_b64 exec, %3\n\tglobal_load_lds_dword %0, %1\n\ts_mov_b64 exec, -1" ::"v"(voff), "s"(sbase), "s"(dst), "s"(lanes) : "memory");
}
DEV void store_wt(unsigned voff, u32x4 srd, unsigned soff, int ofs, u32x4 v) {
  asm volatile("buffer_store_dwordx4 %0, %1, %2, %3 offen offset:%4 sc1\n\ts_nop 1" ::"v"(v), "v"(voff), "s"(srd), "s"(soff), "i"(ofs) : "memory");
}
DEV void store_wt_lane0(unsigned voff, const char* sbase, u32x4 v) {
  asm volatile("s_mov_b64 exec, 1\n\tglobal_store_dwordx4 %0, %1, %2 sc1\n\ts_mov_b64 exec, -1\n\ts_nop 0" ::"v"(voff), "v"(v), "s"(sbase) : "memory");
}
DEV void store_flag(unsigned voff, const char* sbase, u64 v) {
  asm volatile("s_mov_b64 exec, 1\n\tglobal_store_dwordx2 %0, %1, %2 sc1\n\ts_mov_b64 exec, -1" ::"v"(voff), "v"(v), "s"(sbase) : "memory");
}

DEV bf16x8 lds_b128(unsigned a) { return *(LAS bf16x8*)(size_t)a; }
DEV f32x4 lds_f4(unsigned a) { return *(LAS f32x4*)(size_t)a; }
DEV bf16x8 lds_tr(unsigned a0, unsigned a1) {
  bf16x4 a = __builtin_amdgcn_ds_read_tr16_b64_v4bf16((LAS bf16x4*)(size_t)a0);
  bf16x4 b = __builtin_amdgcn_ds_read_tr16_b64_v4bf16((LAS bf16x4*)(size_t)a1);
  return __builtin_shufflevector(a, b, 0, 1, 2, 3, 4, 5, 6, 7);
}
DEV f32x4 mfma(bf16x8 a, bf16x8 b, f32x4 c) { return __builtin_amdgcn_mfma_f32_16x16x32_bf16(a, b, c, 0, 0, 0); }
template <class A, class B> DEV void mma(f32x4& c, A a, B b, bool first) {
  if (first) asm volatile("v_mfma_f32_16x16x32_bf16 %0, %1, %2, 0" : "=&v"(c) : "v"(a), "v"(b));
  else asm volatile("v_mfma_f32_16x16x32_bf16 %0, %1, %2, %0" : "+v"(c) : "v"(a), "v"(b));
}
DEV unsigned pack2(f32x2 v) { return __builtin_bit_cast(unsigned, __builtin_convertvector(v, bf16x2)); }
DEV float blo(unsigned u) { return __uint_as_float(u << 16); }
DEV float bhi(unsigned u) { return __uint_as_float(u & 0xffff0000u); }
DEV f32x2 lo2(f32x4 v) { return __builtin_shufflevector(v, v, 0, 1); }
DEV f32x2 hi2(f32x4 v) { return __builtin_shufflevector(v, v, 2, 3); }
DEV f32x2 fma2(float a, f32x2 b, f32x2 c) { return __builtin_elementwise_fma(f32x2{a, a}, b, c); }
template <int C> DEV float dpp(float x) { return __builtin_bit_cast(float, __builtin_amdgcn_update_dpp(0, __builtin_bit_cast(int, x), C, 0xF, 0xF, true)); }
DEV float max3(float a, float b, float c) { float r; asm("v_max3_f32 %0, %1, %2, %3" : "=v"(r) : "v"(a), "v"(b), "v"(c)); return r; }
DEV float max3_pad(float a, float b, float c) { float r; asm("v_max3_f32 %0, %1, %2, %3\n\ts_nop 1" : "=v"(r) : "v"(a), "v"(b), "v"(c)); return r; }
template <class F> DEV void rows2(float& a, float& b, F f) {
  auto r = __builtin_amdgcn_permlane16_swap(__float_as_uint(a), __float_as_uint(b), false, false);
  const unsigned x = __float_as_uint(f(__uint_as_float(r[0]), __uint_as_float(r[1])));
  r = __builtin_amdgcn_permlane32_swap(x, x, false, false);
  const unsigned y = __float_as_uint(f(__uint_as_float(r[0]), __uint_as_float(r[1])));
  r = __builtin_amdgcn_permlane16_swap(y, y, false, false);
  a = __uint_as_float(r[0]); b = __uint_as_float(r[1]);
}
template <int I> struct Int { static constexpr int v = I; };
template <int I, int N, class F> DEV void static_for(F f) { if constexpr (I < N) { f(Int<I>{}); static_for<I + 1, N>(f); } }
DEV int mod7(int v) { return v >= 7 ? v - 7 : v; }
constexpr int slot_of(int s) { return (s + 2) % 3 * kSlot; }

struct Stg {
  unsigned vo[6], dx, dp, dw;
  u32x4 xs, ws;
  unsigned xo, wo;
  int ks, kstep;
  DEV void next() { xo = ks * 128; wo = ks * kstep; ks = ks == 6 ? 0 : ks + 1; }
};
struct P2 {
  unsigned va[4], vpad;
  unsigned aw[3][4];
  unsigned sA0, sA2;
  u32x4 s2, so, sa;
  int rot;
  bf16x8 Wf[5][4];
  unsigned flag;
  unsigned fofs;
  DEV void peek(const char* ws) const { dma_dev(fofs, ws, kScr + 1408 + (sA0 >> 12) * kScrW, 3); }
  const char *w2, *wo;
  int rg, ct;
  bool live;
  DEV void init(int lane, int w, const char* ws) {
    const int r7 = lane >> 3;
    const unsigned v0 = r7 * 896 + (((lane & 7) ^ (r7 & 2)) << 4);
#pragma unroll
    for (int i = 0; i < 4; ++i) { va[i] = (v0 ^ ((i & 1) << 6)) + i * (8 * 896 - 1024); OPAQUE(va[i]); }
    s2 = make_srd(w2 + w * (224 * 896) + ct * 128, live ? 224 * 896 : 0);
    sa = make_srd(ws + kWsA2, 32 * 72 * 4096);
    OPAQUE_S(s2); OPAQUE_S(sa);
  }
  DEV void init2(int lane, int w, unsigned own_flag) {
    const int g = lane >> 4, q = (lane >> 2) & 3, p = lane & 3;
    const unsigned a0 = (8 * g + q) * 128 + (p >> 1) * 16 + (p & 1) * 8 + sA0, sx = 2 * (g & 1) + (q >> 1);
#pragma unroll
    for (int i = 0; i < 4; ++i) {
      aw[0][i] = a0 + (i ^ sx) * 32; aw[1][i] = aw[0][i] + (sA2 - sA0); aw[2][i] = aw[0][i] + 98304;
      OPAQUE(aw[0][i]); OPAQUE(aw[1][i]); OPAQUE(aw[2][i]);
    }
    vpad = lane < 32 ? va[3] : kOut;
    fofs = kWsFlags + (((rg >> 1) * 16 + w) * 2 + (rg & 1)) * 128 + (lane & 1) * 16;
    flag = own_flag;
    OPAQUE(vpad); OPAQUE(fofs); OPAQUE(flag);
    so = make_srd(wo + w * (56 * 896) + ct * 128, live ? 56 * 896 : 0);
    OPAQUE_S(so);
    PIN();
  }
  DEV int kstep(int s) const { return mod7(s + rot); }
  DEV void m0(int s) const { if (s == 2 || s == 6) set_m0(0, sA2); else set_m0(s == 5 ? 98304 : s == 1 || s == 4 || s == 8 ? kSlot : 0, sA0); }
  DEV void go(int s, int i) const {
    if (s < 7) dma(va[i], s2, kstep(s) * (32 * 896), i * 1024);
    else dma(i == 3 ? vpad : va[i], so, (s - 7) * (28 * 896), i * 1024);
  }
  DEV void ld(int s) const {
    m0(s); NOP(0);
#pragma unroll
    for (int i = 0; i < 4; ++i) go(s, i);
  }
  DEV void rd1(int s, int u) {
    const unsigned a = s == 2 || s == 6 ? aw[1][u] : s == 5 ? aw[2][u] : aw[0][u] + (s == 1 || s == 4 || s == 8 ? kSlot : 0);
    Wf[s % 5][u] = lds_tr(a, a + 512);
  }
  DEV void rd(int s) {
#pragma unroll
    for (int u = 0; u < 4; ++u) rd1(s, u);
  }
};

template <bool FFN, int NT>
DEV void phase1(Stg& sg, const char* b1, char* ws, u64 tag64, int lane, int w, int rt, int cg, P2& p2) {
  constexpr int WOFF = FFN ? 16384 : 18432, WROW = FFN ? 448 : 384;
  const int mh = w & 1, m = lane & 15, g = lane >> 4, q = m >> 2, p = lane & 3;
  const int t0 = FFN ? (NT == 4 ? (w >> 1) * 4 : 8 + ((w >> 1) - 2) * 3) : (w >> 1) * 3;
  auto tile = [&](int i) { return t0 + (NT == 4 ? i ^ (2 * mh) : i == 0 ? 2 * mh : i == 1 ? 1 - mh : 2 - mh); };
  const int r0 = rt * 128;
  auto ld_m0 = [&](int l, unsigned slot) { if (l == 0 || l == 2 || l == 3) set_m0(slot, l == 0 ? sg.dx : l == 2 ? sg.dp : sg.dw); };
  auto ld_go = [&](int l) {
    if (l < 2) dma(sg.vo[l], sg.xs, sg.xo, l * 1024);
    else if (l == 2) dma_part(sg.vo[2], FFN ? sg.ws : sg.xs, FFN ? sg.wo : sg.xo, FFN ? 0xffffffffull : 0xffffull);
    else dma(sg.vo[l], sg.ws, sg.wo, (l - 3) * 1024);
  };
  unsigned ax[2], ae[2], aw[NT];
#pragma unroll
  for (int kk = 0; kk < 2; ++kk) { ax[kk] = slot_of(0) + (64 * mh + m) * 128 + (((4 * kk + g) ^ (m >> 1)) << 4); ae[kk] = ax[kk] + (8 - 4 * mh) * 2048; }
#pragma unroll
  for (int i = 0; i < NT; ++i)
    aw[i] = slot_of(0) + WOFF + (8 * g + q) * WROW + (p >> 1) * 16 + (p & 1) * 8 + (tile(i) ^ (FFN ? g & 1 : 2 * (g & 1) + (q >> 1))) * 32;
  const unsigned scr = kScr + w * kScrW;
  unsigned ast, acx[NT], abi[NT], sto, sso[NT];
  unsigned co[3], ce[2], ak0, ak1, av0, av1, oso;
  f32x2 kb[4];
  u32x4 one = {0x3f803f80u, 0x3f803f80u, 0x3f803f80u, 0x3f803f80u};
  OPAQUE(one);
  const char* first;
  auto setup = [&]() {
    p2.init(lane, w, ws);
    ast = scr + m * 32; OPAQUE(ast);
#pragma unroll
    for (int n = 0; n < NT; ++n) { acx[n] = kCex + (tile(n) * 16 + 4 * g) * 4; OPAQUE(acx[n]); }
    if (FFN) {
      sto = (g & 1) * 1024 + ((g >> 1) * 16 + m) * 16; OPAQUE(sto);
#pragma unroll
      for (int n = 0; n < NT; ++n) {
        abi[n] = scr + 1024 + (tile(n) - t0) * 32 + g * 8; OPAQUE(abi[n]);
        sso[n] = ((2 * rt + mh) * 72 + 7 * cg + (tile(n) >> 1)) * 4096 + (tile(n) & 1) * 512; OPAQUE_S(sso[n]);
      }
      const int l0 = ((rt * 8 + cg) * 8 + w) * 16;
      first = (l0 < 12544 ? p2.w2 : p2.wo) + (l0 < 12544 ? l0 : l0 < 15680 ? l0 - 12544 : 0) * 128;
      OPAQUE_S(first);
    }
    PIN();
  };
  auto prep = [&](int i) {
    if (FFN) return;
    const int blk = w >> 1, par = w & 1;
    const unsigned rb = kAtt + (par * 72 + 16 * blk) * 400;
    if (i < 3) {
      const int T = tile(i), n = 16 * (T & 3) + 4 * g;
      const unsigned c = kAtt + (T >> 2) * 128 + (n + (n < 28 ? 0 : n < 56 ? 4 : n < 60 ? -28 : 0)) * 2;
      co[i] = c + (8 + 32 * mh + m) * 400; OPAQUE(co[i]);
      if (i < 2) { ce[i] = c + ((m & 1) * 72 + (m >> 1)) * 400; OPAQUE(ce[i]); }
    } else if (i == 3) { ak0 = rb + m * 400 + 16 * g; ak1 = rb + (16 + (m & 7)) * 400 + 128 + 16 * g; OPAQUE(ak0); OPAQUE(ak1); }
    else if (i == 4) { av0 = rb + (4 * g + q) * 400 + 256 + 16 * p; av1 = rb + (16 + 4 * (g & 1) + q) * 400 + 256 + 16 * p; OPAQUE(av0); OPAQUE(av1); }
    else if (i < 9) {
      const int k = i - 5, d = m + 8 - 4 * g - (16 * (k >> 1) + 2 * (k & 1));
      kb[k] = f32x2{(unsigned)d < 8u ? 0.f : -__builtin_inff(), (unsigned)(d - 1) < 8u ? 0.f : -__builtin_inff()};
      OPAQUE(kb[k]);
    }
  };
  oso = (((2 * rt + (w >> 2)) * 72 + 56 + 2 * cg) * 4 + (w & 3)) * 1024;
  const unsigned tag = (unsigned)tag64;
  f32x4 acc[4][NT], ace[2];
  bf16x8 X[2][4], W[2][NT], XE[2];
  constexpr int NRD = NT + 2 + (FFN ? 0 : 1);
  auto rd1 = [&](int kk, int i) {
    if (i == 0) { X[kk][0] = lds_b128(ax[kk]); X[kk][1] = lds_b128(ax[kk] + 2048); }
    else if (i == 2) { X[kk][2] = lds_b128(ax[kk] + 4096); X[kk][3] = lds_b128(ax[kk] + 6144); }
    else if (i < NT + 2) { const int n = i == 1 ? 0 : i - 2; W[kk][n] = lds_tr(aw[n] + kk * 32 * WROW, aw[n] + (kk * 32 + 4) * WROW); }
    else if (i < NRD) XE[kk] = lds_b128(ae[kk]);
  };
  auto half = [&](int kk, bool first, int adv, auto load_at, unsigned slot, auto hook) {
    u32x4 e = one;
#pragma unroll
    for (int i = 0; i < 4 * NT + 2; ++i) {
      const int l = load_at(i);
      if (l >= 100) { if ((l & 3) == 0) p2.m0((l - 100) >> 2); } else if (l >= 0) ld_m0(l, slot);
      const int mt = (i & 4) ? 3 - (i & 3) : i & 3;
      if (i < 4 * NT) mma(acc[mt][i >> 2], W[kk][i >> 2], X[kk][mt], first);
      else mma(ace[i & 1], W[kk][i & 1], e, first);
      if (l >= 100) p2.go((l - 100) >> 2, l & 3); else if (l >= 0) ld_go(l);
      if (i % 2 == 0 && i / 2 < NRD) {
        const int r = i / 2;
        if (adv) {
          if (r == 0) { ax[0] += adv; OPAQUE(ax[0]); }
          else if (r == 2) { ax[1] += adv; OPAQUE(ax[1]); }
          else if (r < NT + 2) { const int n = r == 1 ? 0 : r - 2; aw[n] += adv; OPAQUE(aw[n]); }
          else { ae[0] += adv; ae[1] += adv; OPAQUE(ae[0]); OPAQUE(ae[1]); }
        }
        rd1(1 - kk, r);
      }
      if (!FFN && i == 4 * NT - 3) { e = m == 0 ? one : __builtin_bit_cast(u32x4, XE[kk]); OPAQUE(e); }
      hook(i);
      PIN();
    }
  };
  auto ldstats = [&](int i) {
    if (i == 0) dma_dev(lane * 16, ws + kWsStats + (r0 + 64 * mh) * 16, scr, ~0ull);
    else if (FFN) dma_dev(lane * 16, b1 + (cg * 14 + t0) * 32, scr + 1024, (1ull << (2 * NT)) - 1);
    else dma_dev(rt == 0 ? 0 : lane * 16, ws + kWsStats + (rt == 0 ? 0 : r0 - 16) * 16, scr + 1024, 0xffffull);
  };
  u32x4 st[5];
  u32x2 bias[NT];
  f32x4 cs[NT];
  unsigned bad = 0;
  auto rdstats = [&](int i) { st[i] = *(LAS u32x4*)(size_t)(i < 4 ? ast + (i >> 1) * 512 + (i & 1) * 16 : scr + 1024 + m * 16); };
  auto check = [&](int i) { bad |= (st[i][1] ^ tag) | (st[i][3] ^ tag); OPAQUE(bad); };
  constexpr int NST = FFN ? 4 : 5;

  WAIT_VM(8);
  BARRIER();
#pragma unroll
  for (int i = 0; i < NRD; ++i) rd1(0, i);
  sg.next();
#pragma unroll
  for (int l = 0; l < 3; ++l) { ld_m0(l, slot_of(2)); NOP(0); ld_go(l); }
  PIN();
#pragma unroll
  for (int s = 0; s < 6; ++s) {
    auto three = [](int l0) { return [=](int i) { return i % 4 == 1 && i < 12 ? l0 + i / 4 : -1; }; };
    auto four = [](int l0) { return [=](int i) { return i % 3 == 1 && i < 12 ? l0 + i / 3 : -1; }; };
    auto none = [](int) { return -1; };
    if (s < 5) half(0, s == 0, 0, three(3), slot_of(s + 2), [&](int i) { if (s == 3 && (i == 11 || i == 13)) ldstats(i == 13); });
    else half(0, false, 0, none, 0, [&](int i) {
      if ((i & 1) && i / 2 < NST) rdstats(i / 2);
      if (i >= 9 && i < 9 + NST) check(i - 9);
    });
    if (s == 0) setup();
    if (FFN && s == 2) touch(lane * 128, first, scr + 1280, 0xffff);
    if (s == 5 && __builtin_expect(__ballot(bad != 0) != 0, 0)) {
#pragma nounroll
      for (int spin = 0; spin < kSpinCap; ++spin) {
        __builtin_amdgcn_s_sleep(2);
        ldstats(0);
        if (!FFN) ldstats(1);
        WAIT_VM(0);
        bad = 0;
#pragma unroll
        for (int i = 0; i < NST; ++i) { rdstats(i); check(i); }
        if (__ballot(bad != 0) == 0) break;
      }
    }
    WAIT_LGKM0();
    if (s < 5) WAIT_VM(6 + (FFN && s == 2) + (s == 3 ? 2 : 0));
    else WAIT_VM(4);
    BARRIER();
    if (s < 4) sg.next();
    PIN();
    if (s < 4) half(1, false, slot_of(s + 1) - slot_of(s), three(0), slot_of(s), [&](int i) { if (s >= 1 && i % 4 == 3) prep(3 * (s - 1) + i / 4); });
    else half(1, false, slot_of(s + 1) - slot_of(s), four(100 + 4 * (s - 4)), 0, [&](int) {});
  }
  auto blk4 = [&](int kk, int n, auto hook) {
#pragma unroll
    for (int mt = 0; mt < 4; ++mt) { mma(acc[mt][n], W[kk][n], X[kk][mt], false); hook(mt); PIN(); }
  };
  {
    u32x4 e0 = one, e1 = one;
    blk4(0, 0, [&](int i) { rd1(1, i == 0 ? 1 : i == 1 ? 3 : i == 2 ? (FFN ? 0 : NRD - 1) : (FFN ? 2 : 0)); });
    if (!FFN) { e0 = m == 0 ? one : __builtin_bit_cast(u32x4, XE[0]); OPAQUE(e0); }
    blk4(0, 1, [&](int i) {
      if (FFN) { if (i + 4 < NRD) rd1(1, i + 4); bias[i < NT ? i : 0] = *(LAS u32x2*)(size_t)abi[i < NT ? i : 0]; }
      else if (i < 2) rd1(1, i == 0 ? 2 : 4);
    });
    mma(ace[0], W[0][0], e0, false); mma(ace[1], W[0][1], e0, false);
    if (!FFN) { e1 = m == 0 ? one : __builtin_bit_cast(u32x4, XE[1]); OPAQUE(e1); NOP(1); }
    mma(ace[0], W[1][0], e1, false); mma(ace[1], W[1][1], e1, false);
    PIN();
    blk4(0, 2, [&](int) {});
    NOP(3);
    asm volatile("s_mov_b64 exec, %4\n\tds_write_b128 %0, %1\n\tds_write_b128 %2, %3\n\ts_mov_b64 exec, -1\n\ts_waitcnt lgkmcnt(0)"
                 ::"v"(acx[0]), "v"(ace[0]), "v"(acx[1]), "v"(ace[1]), "s"(0x0001000100010001ull) : "memory");
  }
  WAIT_LGKM0();
  BARRIER();
  p2.m0(2);
  blk4(1, 0, [&](int i) { p2.go(2, i); if (i < NT) cs[i] = lds_f4(acx[i]); });
  blk4(1, 1, [&](int) {});
  float rs[5], mu[5];
#pragma unroll
  for (int i = 0; i < NST; ++i) { rs[i] = __uint_as_float(st[i][2]); mu[i] = FFN ? -rs[i] * __uint_as_float(st[i][0]) : -__uint_as_float(st[i][0]); }
  auto epi = [&](int n) {
    if (FFN) {
      const f32x2 b0 = {blo(bias[n][0]), bhi(bias[n][0])}, b1v = {blo(bias[n][1]), bhi(bias[n][1])};
#pragma unroll
      for (int pp = 0; pp < 2; ++pp) {
        unsigned y[2][2];
#pragma unroll
        for (int e = 0; e < 2; ++e) {
          const int mt = 2 * pp + e;
          f32x2 u0 = fma2(rs[mt], lo2(acc[mt][n]), fma2(mu[mt], lo2(cs[n]), b0)), u1 = fma2(rs[mt], hi2(acc[mt][n]), fma2(mu[mt], hi2(cs[n]), b1v));
          u0 = f32x2{fmaxf(u0[0], 0.f), fmaxf(u0[1], 0.f)}; u1 = f32x2{fmaxf(u1[0], 0.f), fmaxf(u1[1], 0.f)};
          y[e][0] = pack2(u0 * u0); y[e][1] = pack2(u1 * u1);
        }
        const auto s0 = __builtin_amdgcn_permlane16_swap(y[0][0], y[1][0], false, false);
        const auto s1 = __builtin_amdgcn_permlane16_swap(y[0][1], y[1][1], false, false);
        store_wt(sto, p2.sa, sso[n], pp * 2048, u32x4{s0[0], s1[0], s0[1], s1[1]});
      }
    } else {
      auto put = [&](const f32x4& a, int i, unsigned addr) {
        *(LAS u32x2*)(size_t)addr = u32x2{pack2(fma2(mu[i], lo2(cs[n]), lo2(a)) * rs[i]), pack2(fma2(mu[i], hi2(cs[n]), hi2(a)) * rs[i])};
      };
      if (n < 2) put(ace[n], 4, ce[n]);
#pragma unroll
      for (int mt = 0; mt < 4; ++mt) put(acc[mt][n], mt, co[n] + ((mt & 1) * 72 + 16 * (mt >> 1)) * 400);
    }
    PIN();
  };
  PIN();
  NOP(3);
  PIN();
  epi(0);
  blk4(1, 2, [&](int) {});
  epi(1);
  if (NT == 4) { blk4(0, 3, [&](int) {}); blk4(1, 3, [&](int) {}); }
  epi(2);
  if (NT == 4) epi(3);
  p2.init2(lane, w, kWsFlags + ((rt * 16 + cg + (FFN ? 0 : 8)) * 2 + (FFN ? w & 1 : w >> 2)) * 128 + (FFN ? w >> 1 : w & 3) * 8);
  if (FFN) {
    WAIT_VM(0);
    store_flag(p2.flag, ws, tag64);
    p2.rd(0);
    p2.rd(1);
    p2.rd(2);
    WAIT_LGKM0();
    BARRIER();
    p2.ld(3); p2.ld(4); p2.ld(5);
    p2.ld(6);
    WAIT_VM(12); p2.rd(3); WAIT_LGKM0();
    p2.ld(7);
    WAIT_VM(12); p2.rd(4);
    p2.peek(ws);
    WAIT_LGKM0();
    p2.ld(8);
  } else {
    WAIT_VM(4);
    p2.rd(0);
    p2.rd(1);
    WAIT_LGKM0();
    BARRIER();
    p2.ld(3);
    const f32x4 z = {0.f, 0.f, 0.f, 0.f};
    f32x4 sc[2][2];
    bf16x8 qf[2], kf[2][2], vf[2][2];
#pragma unroll
    for (int hd = 0; hd < 2; ++hd) { qf[hd] = lds_b128(ak0 + 8 * 400 + hd * 64); kf[hd][0] = lds_b128(ak0 + 128 + hd * 64); kf[hd][1] = lds_b128(ak1 + hd * 64); }
#pragma unroll
    for (int hd = 0; hd < 2; ++hd) { vf[hd][0] = lds_tr(av0 + hd * 64, av1 + hd * 64); vf[hd][1] = lds_tr(av0 + hd * 64 + 8, av1 + hd * 64 + 8); }
    PIN();
#pragma unroll
    for (int hd = 0; hd < 2; ++hd) { sc[hd][0] = mfma(kf[hd][0], qf[hd], z); sc[hd][1] = mfma(kf[hd][1], qf[hd], z); }
    PIN();
    p2.ld(4);
    WAIT_VM(8);
    p2.rd(2);
    WAIT_LGKM0();
    BARRIER();
    p2.ld(5);
    PIN();
    f32x2 e[2][4];
    float mx[2], sum[2];
    const float kLog = 0.18898223650461362f * 1.4426950408889634f;
#pragma unroll
    for (int hd = 0; hd < 2; ++hd) {
#pragma unroll
      for (int t = 0; t < 2; ++t) {
        e[hd][2 * t] = fma2(kLog, lo2(sc[hd][t]), kb[2 * t]);
        e[hd][2 * t + 1] = fma2(kLog, hi2(sc[hd][t]), kb[2 * t + 1]);
      }
    }
    if (__builtin_expect(r0 + 32 * (w >> 1) == 0, 0)) {
#pragma unroll
      for (int i = 0; i < 2; ++i) {
        const f32x2 v = kb[i] + -1.0e9f * 1.4426950408889634f;
        if (g < 2) e[0][i] = e[1][i] = v;
      }
    }
#pragma unroll
    for (int hd = 0; hd < 2; ++hd) {
      mx[hd] = max3_pad(max3(e[hd][0][0], e[hd][0][1], e[hd][1][0]), max3(e[hd][1][1], e[hd][2][0], e[hd][2][1]), max3(e[hd][3][0], e[hd][3][1], e[hd][3][1]));
    }
    rows2(mx[0], mx[1], [](float a, float b) { return max3_pad(a, b, b); });
    PIN();
    p2.ld(6);
    PIN();
#pragma unroll
    for (int hd = 0; hd < 2; ++hd) {
#pragma unroll
      for (int i = 0; i < 4; ++i) { const f32x2 d = e[hd][i] - mx[hd]; e[hd][i] = f32x2{__builtin_amdgcn_exp2f(d[0]), __builtin_amdgcn_exp2f(d[1])}; }
      const f32x2 t = (e[hd][0] + e[hd][1]) + (e[hd][2] + e[hd][3]);
      sum[hd] = t[0] + t[1];
    }
    rows2(sum[0], sum[1], [](float a, float b) { return a + b; });
    f32x4 o[2][2];
#pragma unroll
    for (int hd = 0; hd < 2; ++hd) {
      const float inv = __builtin_amdgcn_rcpf(sum[hd]);
      const u32x4 pf = {pack2(e[hd][0] * inv), pack2(e[hd][1] * inv), pack2(e[hd][2] * inv), pack2(e[hd][3] * inv)};
      o[hd][0] = mfma(vf[hd][0], __builtin_bit_cast(bf16x8, pf), z);
      o[hd][1] = mfma(vf[hd][1], __builtin_bit_cast(bf16x8, pf), z);
    }
    WAIT_VM(8);
    p2.rd(3);
    p2.rd(4);
#pragma unroll
    for (int hd = 0; hd < 2; ++hd)
      store_wt(lane * 16, p2.sa, oso + hd * 4096, 0, u32x4{pack2(lo2(o[hd][0])), pack2(hi2(o[hd][0])), pack2(lo2(o[hd][1])), pack2(hi2(o[hd][1]))});
    WAIT_LGKM0();
    p2.ld(7);
    WAIT_VM(4);
    store_flag(p2.flag, ws, tag64);
    p2.peek(ws);
    p2.ld(8);
  }
}

__global__ __launch_bounds__(512, 2) void fused_kernel(const char*, const char*, const char*, const char*, const char*, const char*, const char*, char*, char*) {
  u32x16 ka;
  u64 kb;
  asm volatile("s_load_dwordx16 %0, %2, 0x0\n\ts_load_dwordx2 %1, %2, 0x40" : "=&s"(ka), "=&s"(kb) : "s"(__builtin_amdgcn_kernarg_segment_ptr()));
  __shared__ __attribute__((aligned(16))) char smem[kLds];
  asm volatile("" ::"v"((LAS char*)smem));
  const int lane = threadIdx.x & 63, w = __builtin_amdgcn_readfirstlane(threadIdx.x >> 6);
  const int bid = blockIdx.x, xcd = bid & 7, j = bid >> 3;
  const int rt = (xcd & 1) * 8 + (j & 7), cgi = j >> 3, cg = 2 * (xcd >> 1) + (cgi & 1);
  const bool ffn = cgi < 2;
  const char *x, *wo, *b1, *w2, *b2;
  char *out, *ws;
  Stg sg;
  u64 tag64;
  {
    const int r0 = rt * 128;
#pragma unroll
    for (int u = 0; u < 2; ++u) {
      const unsigned r = 8 * u + (lane >> 3);
      sg.vo[u] = __umul24(r0 + 32 * (w >> 1) + 2 * r + (w & 1), 896) + (((lane & 7) ^ (r >> 1)) << 4) - u * 1024;
    }
    sg.dx = w * 2048;
    sg.ks = mod7(mod7((rt & 3) * 2 + 2 * (xcd >> 1)));
    asm volatile("" : "+v"(sg.vo[0]), "+v"(sg.vo[1]), "+s"(sg.ks), "+s"(sg.dx));
    asm volatile("s_waitcnt lgkmcnt(0)" : "+s"(ka), "+s"(kb));
    auto ptr = [&](int i) { return (char*)(((u64)ka[2 * i + 1] << 32) | ka[2 * i]); };
    x = ptr(0); wo = ptr(2); b1 = ptr(4); w2 = ptr(5); b2 = ptr(6); out = ptr(7); ws = (char*)kb;
    sg.xs = make_srd(x, 2048 * 896);
    set_m0(slot_of(0), sg.dx);
    NOP(0);
    dma(sg.vo[0], sg.xs, sg.ks * 128, 0);
    dma(sg.vo[1], sg.xs, sg.ks * 128, 1024);
    PIN();
    const unsigned q0 = 48 * w + (lane >> 2), l3 = (lane & 3) << 4;
    if (ffn) {
      auto woff = [&](unsigned Q) { const unsigned k = __umul24(Q, 9363) >> 16; return __umul24(k, 3584 - 448) + Q * 64 + cg * 448 + (l3 ^ ((k << 2) & 32)); };
#pragma unroll
      for (int u = 0; u < 3; ++u) sg.vo[3 + u] = woff(q0 + 16 * u) - u * 1024;
      sg.vo[2] = woff(384 + 8 * w + ((lane >> 2) & 7));
    } else {
#pragma unroll
      for (int u = 0; u < 3; ++u) {
        const unsigned Q = q0 + 16 * u, k = __umul24(Q, 10923) >> 16, cq = Q - 6 * k, c = (((cq & 1) << 6) | l3) ^ ((k & 2) << 4) ^ ((k & 8) << 3);
        sg.vo[3 + u] = (c == 112 ? kOut : __umul24(k, 2688) + __umul24(cq >> 1, 896) + cg * 112 + c) - u * 1024;
      }
      const int hr = r0 - 16 + 2 * w + ((lane >> 3) & 1);
      sg.vo[2] = __umul24(hr < 0 ? 0 : hr, 896) + (((lane & 7) ^ w) << 4);
    }
    sg.dw = (ffn ? 16384 : 18432) + w * 3072;
    sg.dp = ffn ? 16384 + 24576 + w * 512 : 16384 + w * 256;
    sg.kstep = ffn ? 64 * 3584 : 64 * 2688;
    sg.ws = make_srd(ffn ? ptr(3) : ptr(1), 7 * sg.kstep);
    const u32x4 ps = ffn ? sg.ws : sg.xs;
    const u64 part = ffn ? 0xffffffffull : 0xffffull;
    auto ld = [&](int s, int l) {
      if (l == 0 || l == 2 || l == 3) set_m0(slot_of(s), l == 0 ? sg.dx : l == 2 ? sg.dp : sg.dw);
      if (l == 0 || l == 3) NOP(0);
      if (l < 2) dma(sg.vo[l], sg.xs, sg.xo, l * 1024);
      else if (l == 2) dma_part(sg.vo[2], ps, ffn ? sg.wo : sg.xo, part);
      else dma(sg.vo[l], sg.ws, sg.wo, (l - 3) * 1024);
    };
    const int strow = (xcd & 1) * 1024 + (xcd >> 1) * 256 + j * 8 + w;
    u32x2 ep;
    u32x4 srow;
    asm volatile("s_mov_b64 exec, 1\n\tglobal_load_dwordx2 %0, %1, %2 sc1\n\ts_mov_b64 exec, -1" : "=v"(ep) : "v"(0), "s"(ws) : "memory");
    asm volatile("buffer_load_dwordx4 %0, %1, %2, %3 offen" : "=v"(srow) : "v"(lane < 56 ? lane * 16 : kOut), "s"(sg.xs), "s"(strow * 896) : "memory");
    sg.next();
    ld(0, 3); ld(0, 4); ld(0, 5); ld(0, 2);
    {
      const char* pc;
      unsigned nl;
      asm volatile("s_getpc_b64 %0\n.Lfk_pc:\n\ts_mov_b32 %1, (.Lfk_end - .Lfk_pc) / 128" : "=s"(pc), "=s"(nl));
      touch((min((unsigned)(j * 8 + w) * 2, nl - 2) + (lane & 1)) * 128, pc, kScr + w * kScrW + 1280, 3);
    }
    asm volatile("s_waitcnt vmcnt(5)" : "+v"(srow), "+v"(ep)::"memory");
    tag64 = (((u64)__builtin_amdgcn_readfirstlane(ep[1]) << 32) | (unsigned)__builtin_amdgcn_readfirstlane(ep[0])) + 1;
    float sm = 0.f, sq = 0.f;
#pragma unroll
    for (int i = 0; i < 4; ++i) {
      const unsigned u = srow[i];
      const bf16x2 v = __builtin_bit_cast(bf16x2, u);
      sm = __builtin_amdgcn_fdot2_f32_bf16(v, __builtin_bit_cast(bf16x2, 0x3f803f80u), sm, false);
      sq = __builtin_amdgcn_fdot2_f32_bf16(v, v, sq, false);
    }
    sm += dpp<0xB1>(sm); sq += dpp<0xB1>(sq);
    sm += dpp<0x4E>(sm); sq += dpp<0x4E>(sq);
    sm += dpp<0x141>(sm); sq += dpp<0x141>(sq);
    sm += dpp<0x140>(sm); sq += dpp<0x140>(sq);
    rows2(sm, sq, [](float a, float b) { return a + b; });
    sm *= 1.f / 448.f;
    store_wt_lane0(kWsStats + strow * 16, ws,
                   u32x4{__float_as_uint(sm), (unsigned)tag64, __float_as_uint(__builtin_amdgcn_rsqf(fmaxf(fmaf(-sm, sm, sq * (1.f / 448.f)), 0.f) + 1e-5f)), (unsigned)tag64});
    sg.next();
#pragma unroll
    for (int l = 0; l < 6; ++l) ld(1, l);
  }
  const int rg = 4 * xcd + (j & 3), ct = mod7(mod7(7 + (j < 28 ? j >> 2 : 6) - 2 * (j & 3)));
  const int m = lane & 15, g = lane >> 4;
  P2 p2;
  p2.sA0 = w * 4096; p2.sA2 = (w < 3 ? 32768 : 65536) + w * 4096; p2.rot = 2 * (rg & 3);
  p2.w2 = w2; p2.wo = wo; p2.rg = rg; p2.ct = ct; p2.live = j < 28;

  if (!ffn) phase1<false, 3>(sg, b1, ws, tag64, lane, w, rt, cg, p2);
  else if (w < 4) phase1<true, 4>(sg, b1, ws, tag64, lane, w, rt, cg, p2);
  else phase1<true, 3>(sg, b1, ws, tag64, lane, w, rt, cg, p2);

  if (j >= 28) {
    if (bid == 255) {
      const unsigned t0 = (unsigned)tag64, t1 = (unsigned)(tag64 >> 32);
#pragma nounroll
      for (int spin = 0; spin < kSpinCap; ++spin) {
        u32x4 f, h;
        asm volatile("global_load_dwordx4 %0, %2, %3 sc1\n\tglobal_load_dwordx4 %1, %2, %4 sc1\n\ts_waitcnt vmcnt(0)" : "=&v"(f), "=&v"(h)
                     : "v"((lane >> 1) * 128 + (lane & 1) * 16), "s"(ws + kWsFlags + w * 8192), "s"(ws + kWsFlags + w * 8192 + 4096) : "memory");
        if (__all(f[0] == t0 && f[1] == t1 && f[2] == t0 && f[3] == t1 && h[0] == t0 && h[1] == t1 && h[2] == t0 && h[3] == t1)) break;
        __builtin_amdgcn_s_sleep(8);
      }
      BARRIER();
      if (w == 0) store_flag(0, ws, tag64);
    }
    return;
  }

  const unsigned fofs = kWsFlags + (((rg >> 1) * 16 + w) * 2 + (rg & 1)) * 128 + (lane & 3) * 8;
  auto ready = [&](u32x2 f) { return (__ballot(f[0] == (unsigned)tag64 && f[1] == (unsigned)(tag64 >> 32)) & 15) == 15; };
  auto poll = [&](unsigned ofs) {
#pragma nounroll
    for (int spin = 0; spin < kSpinCap; ++spin) {
      u32x2 f;
      asm volatile("s_mov_b64 exec, 15\n\tglobal_load_dwordx2 %0, %1, %2 sc1\n\ts_mov_b64 exec, -1\n\ts_waitcnt vmcnt(0)" : "=&v"(f) : "v"(ofs), "s"(ws) : "memory");
      if (ready(f)) break;
      __builtin_amdgcn_s_sleep(1);
    }
  };
  BARRIER();
  WAIT_VM(4);
  if (!ready(*(LAS u32x2*)(size_t)(kScr + 1408 + w * kScrW + (lane & 3) * 8))) poll(fofs);
  u32x4 H[WIN][4];
  auto hso = [&](int i) { return (unsigned)(rg * 72 + (i < 7 ? 7 * w + p2.kstep(i) : 56 + 2 * w + (i - 7))) * 4096; };
  auto ldh = [&](int i, int mt, unsigned so) {
    asm volatile("buffer_load_dwordx4 %0, %1, %2, %3 offen offset:%4 sc1" : "=v"(H[i % WIN][mt]) : "v"(lane * 16), "s"(p2.sa), "s"(so), "i"(mt * 1024) : "memory");
  };
  asm volatile("s_cmp_lt_u32 %0, 4\n\ts_cbranch_scc1 1f\n\ts_setprio 1\n1:" ::"s"(w) : "scc");
#pragma unroll
  for (int i = 0; i < WIN; ++i) {
    const unsigned so = hso(i);
#pragma unroll
    for (int mt = 0; mt < 4; ++mt) ldh(i, mt, so);
  }
  const int os = threadIdx.x >> 3, orow = rg * 64 + 32 * (os >> 5) + 2 * (os & 15) + ((os >> 4) & 1);
  unsigned oofs = __umul24(orow, 896) + ct * 128 + (threadIdx.x & 7) * 16;
  u32x4 xr, br;
  unsigned pa[3][4], ra[2][2];
#pragma unroll
  for (int u = 0; u < 4; ++u) {
    const unsigned a = m * 256 + (((4 * u + g) ^ (m & 7)) << 4);
    pa[0][u] = a + p2.sA0; pa[1][u] = a + p2.sA0 + 98304; pa[2][u] = a + p2.sA2;
    OPAQUE(pa[0][u]); OPAQUE(pa[1][u]); OPAQUE(pa[2][u]);
  }
  ra[0][0] = (w < 2 ? 0 : w < 4 ? kSlot : w < 6 ? 98304 : 32768) + (os & 15) * 256 + (((2 * (threadIdx.x & 7)) ^ (os & 7)) << 4);
  ra[0][1] = ra[0][0] ^ 16;
  ra[1][0] = ra[0][0] + (w < 6 ? 0 : 32768);
  ra[1][1] = ra[1][0] ^ 16;
  OPAQUE(ra[0][0]); OPAQUE(ra[0][1]); OPAQUE(ra[1][0]); OPAQUE(ra[1][1]); OPAQUE(oofs);
  f32x4 acc[4][4], lo, hi;
  u32x2 fq;
  auto park = [&](int mt, int u) { *(LAS f32x4*)(size_t)(mt < 2 ? pa[0][u] + mt * kSlot : pa[mt - 1][u]) = acc[mt][u]; };
  auto ldfq = [&]() { asm volatile("s_mov_b64 exec, 15\n\tglobal_load_dwordx2 %0, %1, %2 offset:2048 sc1\n\ts_mov_b64 exec, -1" : "=v"(fq) : "v"(fofs), "s"(ws) : "memory"); };
  auto ldxb = [&]() {
    asm volatile("global_load_dwordx4 %0, %1, %2" : "=v"(xr) : "v"(oofs), "s"(x) : "memory");
    asm volatile("global_load_dwordx4 %0, %1, %2" : "=v"(br) : "v"(ct * 128 + (threadIdx.x & 7) * 16), "s"(b2) : "memory");
  };
  static_for<0, 9>([&](auto I) {
    constexpr int i = I.v;
    const unsigned so = i + WIN < 9 ? hso(i + WIN) : 0;
    if (i == 7 - WIN) {
      asm volatile("s_waitcnt vmcnt(6)" : "+v"(fq)::"memory");
      if (!ready(fq)) poll(fofs + 2048);
    }
    static_for<0, 4>([&](auto MT) {
      constexpr int mt = MT.v, left = 4 * (8 - i) + 3 - mt;
      constexpr int younger = (left < 4 * WIN - 1 ? left : 4 * WIN - 1);
      asm volatile("s_waitcnt vmcnt(%1)" : "+v"(H[i % WIN][mt]) : "i"(younger) : "memory");
      static_for<0, 4>([&](auto UU) {
        constexpr int uu = UU.v, u = i < 8 && (mt & 1) ? 3 - uu : uu;
        mma(acc[mt][u], p2.Wf[i % 5][u], H[i % WIN][mt], i == 0);
        if (uu == 0 && i >= 1 && i < 3) p2.rd1(i + 4, mt);
        if (uu == 0 && i >= 4 && i < 6) p2.rd1(i + 3, mt);
        if (i == 7 && mt == 0 && uu == 0) {
          OPAQUE(xr); OPAQUE(br);
          lo = f32x4{blo(xr[0]) + blo(br[0]), bhi(xr[0]) + bhi(br[0]), blo(xr[1]) + blo(br[1]), bhi(xr[1]) + bhi(br[1])};
          hi = f32x4{blo(xr[2]) + blo(br[2]), bhi(xr[2]) + bhi(br[2]), blo(xr[3]) + blo(br[3]), bhi(xr[3]) + bhi(br[3])};
          OPAQUE(lo); OPAQUE(hi);
        }
        if (i == 8 && mt >= 1) park(mt >= 1 ? mt - 1 : 0, u);
        PIN();
      });
      if (i + WIN < 9) ldh(i, mt, so);
    });
    if (i == 5 - WIN) ldfq();
    if (i == 6 - WIN) ldxb();
  });
  NOP(6);
  PIN();
#pragma unroll
  for (int u = 0; u < 4; ++u) park(3, u);
  WAIT_LGKM0();
  BARRIER();
#pragma unroll
  for (int k = 0; k < 8; k += 2) {
    lo += lds_f4(ra[k >= 3][0] + k * 4096) + lds_f4(ra[k + 1 >= 3][0] + (k + 1) * 4096);
    hi += lds_f4(ra[k >= 3][1] + k * 4096) + lds_f4(ra[k + 1 >= 3][1] + (k + 1) * 4096);
  }
  const u32x4 res = {pack2(lo2(lo)), pack2(hi2(lo)), pack2(lo2(hi)), pack2(hi2(hi))};
  asm volatile("global_store_dwordx4 %0, %1, %2 sc1" ::"v"(oofs), "v"(res), "s"(out) : "memory");
  asm volatile(".Lfk_end:");
}

extern "C" void kernel_launch(void* const* d_in, const int* in_sizes, int n_in, void* d_out, int out_size, void* d_ws, size_t ws_size, hipStream_t stream) {
  fused_kernel<<<256, 512, 0, stream>>>((const char*)d_in[0], (const char*)d_in[1], (const char*)d_in[2], (const char*)d_in[4], (const char*)d_in[5],
                                        (const char*)d_in[6], (const char*)d_in[7], (char*)d_out, (char*)d_ws);
}
```
